```python
import torch

def _softmax(x, axis=-1):
    return torch.softmax(x, dim=axis)
def _gelu(x):
    return torch.nn.functional.gelu(x, approximate="tanh")
def _silu(x):
    return torch.nn.functional.silu(x)
def _sigmoid(x):
    return torch.sigmoid(x)
def _layernorm(x, axis=-1, eps=1e-5):
    m = x.mean(dim=axis, keepdim=True)
    v = x.var(dim=axis, keepdim=True, unbiased=False)
    return (x - m) / torch.sqrt(v + eps)
def _scatter_add(tgt, idx, src, axis=0):
    return tgt.clone().index_add_(axis, idx, src)
def _segment_sum(src, seg, n):
    out = torch.zeros((n,) + src.shape[1:], dtype=src.dtype)
    return out.index_add_(0, seg, src)
def _pad(x, pw):
    flat = tuple(v for pair in reversed(pw) for v in pair)
    return torch.nn.functional.pad(x, flat)

kernel_name = 'interleave_layers__gqa_block_cached__mixer_channel_mlp__decode'

def setup_inputs():

    _w = (lambda a: (lambda t: t.to(torch.bfloat16) if t.is_floating_point() else t)(torch.as_tensor(a)))
    def _A_setup():
        B, Sq, D = 1, 1, 640
        Hq, Hkv, Sk, Dh = 32, 32, 4096, 128
        assert Hq % Hkv == 0, (Hq, Hkv)
        return {
            "x": _w((lambda *s: torch.randn(*s, dtype=torch.bfloat16) * 0.05)(B, Sq, D)),
            "k_cache": _w((lambda *s: torch.randn(*s, dtype=torch.bfloat16) * 3.2)(B, Hkv, Sk, Dh)),
            "v_cache": _w((lambda *s: torch.randn(*s, dtype=torch.bfloat16) * 0.4)(B, Hkv, Sk, Dh)),
            "wq": _w((lambda *s: torch.randn(*s, dtype=torch.bfloat16) * 3.2)(D, Hq * Dh) * (D ** -0.5)),
            "wo": _w((lambda *s: torch.randn(*s, dtype=torch.bfloat16) * 0.4)(Hq * Dh, D) * ((Hq * Dh) ** -0.5)),
        }
    _A_in = _A_setup()

    def _B_setup():
        B, S, D, F = 1, 1, 640, 1728
        return {
            "x": _w((lambda *s: torch.randn(*s, dtype=torch.bfloat16) * 0.05)(B, S, D)),
            "w1": _w((lambda *s: torch.randn(*s, dtype=torch.bfloat16) * 0.4)(D, F) * (D ** -0.5)),
            "b1": _w((lambda *s: torch.randn(*s, dtype=torch.bfloat16) * 0.4)(F)),
            "w2": _w((lambda *s: torch.randn(*s, dtype=torch.bfloat16) * 0.4)(F, D) * (F ** -0.5)),
            "b2": _w((lambda *s: torch.randn(*s, dtype=torch.bfloat16) * 0.2)(D)),
        }
    _B_in = _B_setup()

    return {
        "A_x": _A_in["x"],
        "A_k_cache": _A_in["k_cache"],
        "A_v_cache": _A_in["v_cache"],
        "A_wq": _A_in["wq"],
        "A_wo": _A_in["wo"],
        "B_x": _B_in["x"],
        "B_w1": _B_in["w1"],
        "B_b1": _B_in["b1"],
        "B_w2": _B_in["w2"],
        "B_b2": _B_in["b2"],
    }

def reference(A_x, A_k_cache, A_v_cache, A_wq, A_wo, B_x, B_w1, B_b1, B_w2, B_b2):
    def _A_ref(x, k_cache, v_cache, wq, wo):
        B, Sq, D = x.shape
        Hq, Dh = 32, 128
        Hkv = k_cache.shape[1]
        rep = Hq // Hkv
        q = (lambda x, *d: x.permute(*d))((x @ wq).reshape(B, Sq, Hq, Dh), 0, 2, 1, 3)
        k = (lambda x, n, axis: torch.repeat_interleave(x, n, dim=axis))(k_cache, rep, axis=1)
        v = (lambda x, n, axis: torch.repeat_interleave(x, n, dim=axis))(v_cache, rep, axis=1)
        scores = torch.einsum("bhqd,bhkd->bhqk", q, k) * (Dh ** -0.5)
        p = _softmax(scores, axis=-1)
        o = torch.einsum("bhqk,bhkd->bhqd", p, v)
        return (lambda x, *d: x.permute(*d))(o, 0, 2, 1, 3).reshape(B, Sq, Hq * Dh) @ wo

    def _B_ref(x, w1, b1, w2, b2):
        y = _layernorm(x, axis=-1)
        h = _gelu(torch.einsum("bsd,df->bsf", y, w1) + b1)
        return torch.einsum("bsf,fd->bsd", h, w2) + b2

    x = A_x
    x = x + _A_ref(_layernorm(x, axis=-1), A_k_cache, A_v_cache, A_wq, A_wo)
    x = x + _B_ref(_layernorm(x, axis=-1), B_w1, B_b1, B_w2, B_b2)
    x = x + _A_ref(_layernorm(x, axis=-1), A_k_cache, A_v_cache, A_wq, A_wo)
    x = x + _B_ref(_layernorm(x, axis=-1), B_w1, B_b1, B_w2, B_b2)
    return x
```

```cpp
#include <hip/hip_runtime.h>
#include <hip/hip_bf16.h>
#include <stdint.h>
#define MIMIC 1
typedef unsigned short u16;
__device__ __forceinline__ float bf2f(u16 v){ return __uint_as_float(((unsigned)v)<<16); }
__device__ __forceinline__ u16 f2bf(float f){ unsigned u=__float_as_uint(f); return (u16)((u+0x7fffu+((u>>16)&1u))>>16); }
__device__ __forceinline__ float rb(float f){
  return bf2f(f2bf(f));
}
constexpr int D=640,H=32,SK=4096,DH=128,F=1728,NS=8;

__device__ float block_sum(float v, float* red){
  int t=threadIdx.x;
  for(int o=32;o>0;o>>=1) v+=__shfl_xor(v,o);
  __syncthreads();
  if((t&63)==0) red[t>>6]=v;
  __syncthreads();
  float s=0; for(int i=0;i<(int)blockDim.x/64;i++) s+=red[i];
  return s;
}
__device__ void ln(float* xs, float* red){
  int t=threadIdx.x;
  float s=0; for(int i=t;i<D;i+=blockDim.x) s+=xs[i];
  float mean=block_sum(s,red)/D;
  float q=0; for(int i=t;i<D;i+=blockDim.x){float d=xs[i]-mean; q+=d*d;}
  float var=block_sum(q,red)/D;
  float m=rb(mean), v=rb(var);
  float den=rb(sqrtf(rb(v+1e-5f)));
  __syncthreads();
  for(int i=t;i<D;i+=blockDim.x) xs[i]=rb(rb(xs[i]-m)/den);
  __syncthreads();
}
__global__ void k_init(const u16* x, float* xw){ int t=threadIdx.x+blockIdx.x*blockDim.x; if(t<D) xw[t]=bf2f(x[t]); }
__global__ void k_q(const float* xw, const u16* wq, float* q){
  __shared__ float xs[D]; __shared__ float red[16];
  int t=threadIdx.x;
  for(int i=t;i<D;i+=blockDim.x) xs[i]=xw[i];
  __syncthreads();
  ln(xs,red);
  int j=blockIdx.x*128+t;
  float a=0; for(int d=0;d<D;d++) a+=xs[d]*bf2f(wq[(size_t)d*4096+j]);
  q[j]=rb(a);
}
__global__ void k_attn(const float* q, const u16* K, const u16* V, float* o){
  __shared__ float sc[SK]; __shared__ float qs[DH]; __shared__ float red[16]; __shared__ float acc[8][DH];
  int h=blockIdx.x,t=threadIdx.x;
  if(t<DH) qs[t]=q[h*DH+t];
  __syncthreads();
  float mx=-INFINITY;
  for(int k=t;k<SK;k+=1024){
    const u16* kr=K+((size_t)h*SK+k)*DH; float a=0;
    for(int d=0;d<DH;d++) a+=qs[d]*bf2f(kr[d]);
    a=rb(rb(a)*rb(0.088388347648318440f)); sc[k]=a; mx=fmaxf(mx,a);
  }
  for(int o2=32;o2>0;o2>>=1) mx=fmaxf(mx,__shfl_xor(mx,o2));
  __syncthreads();
  if((t&63)==0) red[t>>6]=mx;
  __syncthreads();
  mx=-INFINITY; for(int i=0;i<16;i++) mx=fmaxf(mx,red[i]);
  float s=0;
  for(int k=t;k<SK;k+=1024){ float e=expf(sc[k]-mx); sc[k]=e; s+=e; }
  float L=block_sum(s,red);
  __syncthreads();
  for(int k=t;k<SK;k+=1024) sc[k]=rb(sc[k]/L);
  __syncthreads();
  int d=t&127,g=t>>7; float a=0;
  for(int k=g;k<SK;k+=8) a+=sc[k]*bf2f(V[((size_t)h*SK+k)*DH+d]);
  acc[g][d]=a;
  __syncthreads();
  if(t<DH){ float r=0; for(int i=0;i<8;i++) r+=acc[i][t]; o[h*DH+t]=rb(r); }
}
__global__ void k_wo(const float* o, const u16* wo, float* xw){
  __shared__ float acc[16][64];
  int t=threadIdx.x,c=blockIdx.x*64+(t&63),g=t>>6; float a=0;
  for(int r=g;r<4096;r+=16) a+=o[r]*bf2f(wo[(size_t)r*D+c]);
  acc[g][t&63]=a; __syncthreads();
  if(t<64){ float r=0; for(int i=0;i<16;i++) r+=acc[i][t]; xw[c]=rb(xw[c]+rb(r)); }
}
__global__ void k_m1(const float* xw, const u16* w1, const u16* b1, float* hh){
  __shared__ float xs[D]; __shared__ float red[16];
  int t=threadIdx.x;
  for(int i=t;i<D;i+=blockDim.x) xs[i]=xw[i];
  __syncthreads();
  ln(xs,red); ln(xs,red);
  int j=blockIdx.x*64+t;
  float a=0; for(int d=0;d<D;d++) a+=xs[d]*bf2f(w1[(size_t)d*F+j]);
  a=rb(rb(a)+bf2f(b1[j]));
  float u=0.7978845608028654f*(a+0.044715f*a*a*a);
  hh[j]=rb(0.5f*a*(1.f+tanhf(u)));
}
__global__ void k_m2(const float* hh, const u16* w2, const u16* b2, float* xw, u16* out){
  __shared__ float acc[16][64];
  int t=threadIdx.x,c=blockIdx.x*64+(t&63),g=t>>6; float a=0;
  for(int r=g;r<F;r+=16) a+=hh[r]*bf2f(w2[(size_t)r*D+c]);
  acc[g][t&63]=a; __syncthreads();
  if(t<64){ float r=0; for(int i=0;i<16;i++) r+=acc[i][t]; float v=rb(xw[c]+rb(rb(r)+bf2f(b2[c]))); xw[c]=v; out[c]=f2bf(v); }
}
extern "C" void kernel_launch(void* const* d_in, const int* in_sizes, int n_in,
                              void* d_out, int out_size, void* d_ws, size_t ws_size,
                              hipStream_t stream) {
  const u16* x=(const u16*)d_in[0]; const u16* K=(const u16*)d_in[1]; const u16* V=(const u16*)d_in[2];
  const u16* wq=(const u16*)d_in[3]; const u16* wo=(const u16*)d_in[4];
  const u16* w1=(const u16*)d_in[6]; const u16* b1=(const u16*)d_in[7]; const u16* w2=(const u16*)d_in[8]; const u16* b2=(const u16*)d_in[9];
  float* ws=(float*)d_ws; float* xw=ws; float* q=ws+1024; float* o=ws+1024+4096; float* hh=ws+1024+8192;
  k_init<<<1,640,0,stream>>>(x,xw);
  for(int l=0;l<2;l++){
    k_q<<<32,128,0,stream>>>(xw,wq,q);
    k_attn<<<32,1024,0,stream>>>(q,K,V,o);
    k_wo<<<10,1024,0,stream>>>(o,wo,xw);
    k_m1<<<27,64,0,stream>>>(xw,w1,b1,hh);
    k_m2<<<10,1024,0,stream>>>(hh,w2,b2,xw,(u16*)d_out);
  }
}
```

```cpp
#include <hip/hip_runtime.h>
#include <stdint.h>

typedef unsigned u32;
typedef unsigned long long u64;
typedef u32 u32x4 __attribute__((ext_vector_type(4)));
typedef u32 u32x2 __attribute__((ext_vector_type(2)));
typedef float f32x4 __attribute__((ext_vector_type(4)));
typedef float f32x2 __attribute__((ext_vector_type(2)));
typedef __bf16 bf16x2 __attribute__((ext_vector_type(2)));
typedef __bf16 bf16x8 __attribute__((ext_vector_type(8)));
typedef __amdgpu_buffer_rsrc_t rsrc_t;

#define DEV __device__ __forceinline__
#define CBAR() asm volatile("" ::: "memory")
#define SB() __builtin_amdgcn_sched_barrier(0)

#define DIAG 0

constexpr u32 WS_E = 0;
constexpr u32 WS_FLAGS = 4096;
constexpr u32 WS_LB = 16384;
constexpr u32 WS_LSTRIDE = 0x50000;
constexpr u32 O_REC = 0;
constexpr u32 O_Z = 0x28000;
constexpr u32 O_H = 0x2E000;
constexpr u32 O_X = 0x30000;
constexpr u32 O_Q = 0x40000;
constexpr u32 WS_DIAG = 0x100000;
constexpr u32 SENT = 0xFFFFFFFFu;
constexpr int SPIN_G = 1 << 16;
constexpr int SPIN_L = 1 << 22;
constexpr float LOG2E = 1.4426950408889634f;

DEV float u2f(u32 v) { return __builtin_bit_cast(float, v); }
DEV u32 f2u(float v) { return __builtin_bit_cast(u32, v); }
DEV float dot2(u32 a, u32 b, float c) {
  return __builtin_amdgcn_fdot2_f32_bf16(__builtin_bit_cast(bf16x2, a), __builtin_bit_cast(bf16x2, b), c, false);
}
DEV u32 pk(float lo, float hi) {
  f32x2 t = {lo, hi};
  bf16x2 p = __builtin_convertvector(t, bf16x2);
  return __builtin_bit_cast(u32, p);
}
DEV float rb(float x) { return u2f(pk(0.f, x)); }
DEV float lo2f(u32 p) { return u2f(p << 16); }
DEV float hi2f(u32 p) { return u2f(p & 0xffff0000u); }
template <int CTRL> DEV float dppf(float x) { return u2f((u32)__builtin_amdgcn_mov_dpp((int)f2u(x), CTRL, 0xf, 0xf, true)); }
template <int CTRL> DEV u32 dppu(u32 x) { return (u32)__builtin_amdgcn_mov_dpp((int)x, CTRL, 0xf, 0xf, true); }
constexpr int QP_X1 = 0xB1, QP_X2 = 0x4E, QP_B0 = 0x00, QP_B1 = 0x55, ROR4 = 0x124, ROR8 = 0x128, HMIR = 0x141;
DEV float red32(float a, float b) {
  auto s = __builtin_amdgcn_permlane32_swap(f2u(a), f2u(b), false, false);
  u32 x = s[0], y = s[1];
  return u2f(x) + u2f(y);
}
DEV float red16(float a, float b) {
  auto s = __builtin_amdgcn_permlane16_swap(f2u(a), f2u(b), false, false);
  u32 x = s[0], y = s[1];
  return u2f(x) + u2f(y);
}
DEV float rdlane(float x, int l) { return u2f((u32)__builtin_amdgcn_readlane((int)f2u(x), l)); }
DEV u32 umax3(u32 a, u32 b, u32 c) { u32 m = a > b ? a : b; return m > c ? m : c; }
DEV u32 clampS(u32 x) { return x < 0xFFFFFFFEu ? x : 0xFFFFFFFEu; }
DEV u32 plo(u32 a, u32 b) { return __builtin_amdgcn_perm(b, a, 0x05040100u); }
DEV u32 phi(u32 a, u32 b) { return __builtin_amdgcn_perm(b, a, 0x07060302u); }
DEV u64 rt() { return __builtin_amdgcn_s_memrealtime(); }

DEV u32x4 ld128s(rsrc_t r, u32 v, u32 s) { return __builtin_amdgcn_raw_buffer_load_b128(r, v, s, 16); }
DEV u32x2 ld64s(rsrc_t r, u32 v, u32 s) { return __builtin_amdgcn_raw_buffer_load_b64(r, v, s, 16); }
DEV u32 ld32s(rsrc_t r, u32 v, u32 s) { return __builtin_amdgcn_raw_buffer_load_b32(r, v, s, 16); }
DEV void st128s(u32x4 d, rsrc_t r, u32 v, u32 s) { __builtin_amdgcn_raw_buffer_store_b128(d, r, v, s, 16); }
DEV void st64s(u32x2 d, rsrc_t r, u32 v, u32 s) { __builtin_amdgcn_raw_buffer_store_b64(d, r, v, s, 16); }
DEV void st32s(u32 d, rsrc_t r, u32 v, u32 s) { __builtin_amdgcn_raw_buffer_store_b32(d, r, v, s, 16); }

#define OPAQUE(x) asm volatile("" : "+v"(x))

struct Lds {
  u32 ys[320];
  u32 xs[320];
  u32 x1s[320];
  u32 qs[64];
  float qpart[4][16];
  float mw[2][8];
  float part[8][132];
  float wpart[8][32];
  u32 x1m[8];
  u32 tag;
  u32 f_y, f_q, f_ep, c_q, c_part, c_role, c_x1;
};

DEV void lds_wait(volatile u32* f, u32 v) {
  int sp = 0;
  while (*f < v && ++sp < SPIN_L) {}
  CBAR();
}
DEV u32 lds_inc(u32* c) {
  CBAR();
  u32 r = 0;
  if (__builtin_amdgcn_mbcnt_hi(~0u, __builtin_amdgcn_mbcnt_lo(~0u, 0u)) == 0)
    r = __hip_atomic_fetch_add(c, 1u, __ATOMIC_RELAXED, __HIP_MEMORY_SCOPE_WORKGROUP);
  r = __builtin_amdgcn_readfirstlane(r);
  CBAR();
  return r;
}

#define LN_PASS(p0, p1, p2, p3, p4) do { \
    float s_ = dot2(p0, 0x3f803f80u, 0.f), ss_ = dot2(p0, p0, 0.f); \
    s_ = dot2(p1, 0x3f803f80u, s_); ss_ = dot2(p1, p1, ss_); \
    s_ = dot2(p2, 0x3f803f80u, s_); ss_ = dot2(p2, p2, ss_); \
    s_ = dot2(p3, 0x3f803f80u, s_); ss_ = dot2(p3, p3, ss_); \
    s_ = dot2(p4, 0x3f803f80u, s_); ss_ = dot2(p4, p4, ss_); \
    float t_ = red32(s_, ss_); \
    t_ += dppf<QP_X1>(t_); t_ += dppf<QP_X2>(t_); t_ += dppf<ROR4>(t_); t_ += dppf<ROR8>(t_); \
    const float S_ = rdlane(t_, 0) + rdlane(t_, 16), SS_ = rdlane(t_, 32) + rdlane(t_, 48); \
    const float mean_ = S_ * (1.f / 640.f); \
    const float var_ = fmaxf(__builtin_fmaf(-mean_, mean_, SS_ * (1.f / 640.f)), 0.f); \
    const float m_ = rb(mean_); \
    const float r_ = __builtin_amdgcn_rcpf(rb(__builtin_amdgcn_sqrtf(rb(rb(var_) + 1e-5f)))); \
    p0 = pk(rb(lo2f(p0) - m_) * r_, rb(hi2f(p0) - m_) * r_); \
    p1 = pk(rb(lo2f(p1) - m_) * r_, rb(hi2f(p1) - m_) * r_); \
    p2 = pk(rb(lo2f(p2) - m_) * r_, rb(hi2f(p2) - m_) * r_); \
    p3 = pk(rb(lo2f(p3) - m_) * r_, rb(hi2f(p3) - m_) * r_); \
    p4 = pk(rb(lo2f(p4) - m_) * r_, rb(hi2f(p4) - m_) * r_); \
  } while (0)

#define KLOAD(vo, base) asm volatile( \
  "global_load_dwordx4 v[128:131], %0, %1 offset:-4096 nt\n\t" \
  "global_load_dwordx4 v[132:135], %0, %1 offset:-3968 nt\n\t" \
  "global_load_dwordx4 v[136:139], %0, %1 offset:-3072 nt\n\t" \
  "global_load_dwordx4 v[140:143], %0, %1 offset:-2944 nt\n\t" \
  "global_load_dwordx4 v[144:147], %0, %1 offset:-2048 nt\n\t" \
  "global_load_dwordx4 v[148:151], %0, %1 offset:-1920 nt\n\t" \
  "global_load_dwordx4 v[152:155], %0, %1 offset:-1024 nt\n\t" \
  "global_load_dwordx4 v[156:159], %0, %1 offset:-896 nt\n\t" \
  "global_load_dwordx4 v[160:163], %0, %1 offset:0 nt\n\t" \
  "global_load_dwordx4 v[164:167], %0, %1 offset:128 nt\n\t" \
  "global_load_dwordx4 v[168:171], %0, %1 offset:1024 nt\n\t" \
  "global_load_dwordx4 v[172:175], %0, %1 offset:1152 nt\n\t" \
  "global_load_dwordx4 v[176:179], %0, %1 offset:2048 nt\n\t" \
  "global_load_dwordx4 v[180:183], %0, %1 offset:2176 nt\n\t" \
  "global_load_dwordx4 v[184:187], %0, %1 offset:3072 nt\n\t" \
  "global_load_dwordx4 v[188:191], %0, %1 offset:3200 nt\n\t" \
  :: "v"(vo), "s"(base) : "memory", "v255")
#define VLOAD(vo, b0, b1) asm volatile( \
  "global_load_dwordx4 v[192:195], %0, %1 offset:-4096 nt\n\t" \
  "global_load_dwordx4 v[196:199], %0, %1 offset:-3072 nt\n\t" \
  "global_load_dwordx4 v[200:203], %0, %1 offset:-2048 nt\n\t" \
  "global_load_dwordx4 v[204:207], %0, %1 offset:-1024 nt\n\t" \
  "global_load_dwordx4 v[208:211], %0, %1 offset:0 nt\n\t" \
  "global_load_dwordx4 v[212:215], %0, %1 offset:1024 nt\n\t" \
  "global_load_dwordx4 v[216:219], %0, %1 offset:2048 nt\n\t" \
  "global_load_dwordx4 v[220:223], %0, %1 offset:3072 nt\n\t" \
  "global_load_dwordx4 v[224:227], %0, %2 offset:-4096 nt\n\t" \
  "global_load_dwordx4 v[228:231], %0, %2 offset:-3072 nt\n\t" \
  "global_load_dwordx4 v[232:235], %0, %2 offset:-2048 nt\n\t" \
  "global_load_dwordx4 v[236:239], %0, %2 offset:-1024 nt\n\t" \
  "global_load_dwordx4 v[240:243], %0, %2 offset:0 nt\n\t" \
  "global_load_dwordx4 v[244:247], %0, %2 offset:1024 nt\n\t" \
  "global_load_dwordx4 v[248:251], %0, %2 offset:2048 nt\n\t" \
  "global_load_dwordx4 v[252:255], %0, %2 offset:3072 nt\n\t" \
  :: "v"(vo), "s"(b0), "s"(b1) : "memory", "v255")
#define WQLOAD(o0, o1, base) asm volatile( \
  "global_load_dwordx2 v[192:193], %0, %2\n\t" \
  "v_add_u32 %0, 0x2000, %0\n\t" \
  "global_load_dwordx2 v[194:195], %0, %2\n\t" \
  "v_add_u32 %0, 0x2000, %0\n\t" \
  "global_load_dwordx2 v[196:197], %0, %2\n\t" \
  "v_add_u32 %0, 0x2000, %0\n\t" \
  "global_load_dwordx2 v[198:199], %0, %2\n\t" \
  "v_add_u32 %0, 0x2000, %0\n\t" \
  "global_load_dwordx2 v[200:201], %0, %2\n\t" \
  "v_add_u32 %0, 0x2000, %0\n\t" \
  "global_load_dwordx2 v[202:203], %0, %2\n\t" \
  "v_add_u32 %0, 0x2000, %0\n\t" \
  "global_load_dwordx2 v[204:205], %0, %2\n\t" \
  "v_add_u32 %0, 0x2000, %0\n\t" \
  "global_load_dwordx2 v[206:207], %0, %2\n\t" \
  "global_load_dwordx2 v[208:209], %1, %2\n\t" \
  "v_add_u32 %1, 0x2000, %1\n\t" \
  "global_load_dwordx2 v[210:211], %1, %2\n\t" \
  : "+v"(o0), "+v"(o1) : "s"(base) : "memory", "v255")
#define WQPERM() asm volatile( \
  "s_waitcnt vmcnt(16)\n\t" \
  "v_perm_b32 v68, v194, v192, %0\n\t" \
  "v_perm_b32 v69, v194, v192, %1\n\t" \
  "v_perm_b32 v70, v195, v193, %0\n\t" \
  "v_perm_b32 v71, v195, v193, %1\n\t" \
  "v_perm_b32 v72, v198, v196, %0\n\t" \
  "v_perm_b32 v73, v198, v196, %1\n\t" \
  "v_perm_b32 v74, v199, v197, %0\n\t" \
  "v_perm_b32 v75, v199, v197, %1\n\t" \
  "v_perm_b32 v76, v202, v200, %0\n\t" \
  "v_perm_b32 v77, v202, v200, %1\n\t" \
  "v_perm_b32 v78, v203, v201, %0\n\t" \
  "v_perm_b32 v79, v203, v201, %1\n\t" \
  "v_perm_b32 v80, v206, v204, %0\n\t" \
  "v_perm_b32 v81, v206, v204, %1\n\t" \
  "v_perm_b32 v82, v207, v205, %0\n\t" \
  "v_perm_b32 v83, v207, v205, %1\n\t" \
  "v_perm_b32 v84, v210, v208, %0\n\t" \
  "v_perm_b32 v85, v210, v208, %1\n\t" \
  "v_perm_b32 v86, v211, v209, %0\n\t" \
  "v_perm_b32 v87, v211, v209, %1\n\t" \
  :: "s"(0x05040100u), "s"(0x07060302u) : "memory")
#define QGEMV(a0, a1, a2, a3, y0, y1, y2, y3, y4) asm volatile( \
  "v_dot2_f32_bf16 %0, v68, %4, 0\n\t" \
  "v_dot2_f32_bf16 %1, v69, %4, 0\n\t" \
  "v_dot2_f32_bf16 %2, v70, %4, 0\n\t" \
  "v_dot2_f32_bf16 %3, v71, %4, 0\n\t" \
  "v_dot2_f32_bf16 %0, v72, %5, %0\n\t" \
  "v_dot2_f32_bf16 %1, v73, %5, %1\n\t" \
  "v_dot2_f32_bf16 %2, v74, %5, %2\n\t" \
  "v_dot2_f32_bf16 %3, v75, %5, %3\n\t" \
  "v_dot2_f32_bf16 %0, v76, %6, %0\n\t" \
  "v_dot2_f32_bf16 %1, v77, %6, %1\n\t" \
  "v_dot2_f32_bf16 %2, v78, %6, %2\n\t" \
  "v_dot2_f32_bf16 %3, v79, %6, %3\n\t" \
  "v_dot2_f32_bf16 %0, v80, %7, %0\n\t" \
  "v_dot2_f32_bf16 %1, v81, %7, %1\n\t" \
  "v_dot2_f32_bf16 %2, v82, %7, %2\n\t" \
  "v_dot2_f32_bf16 %3, v83, %7, %3\n\t" \
  "v_dot2_f32_bf16 %0, v84, %8, %0\n\t" \
  "v_dot2_f32_bf16 %1, v85, %8, %1\n\t" \
  "v_dot2_f32_bf16 %2, v86, %8, %2\n\t" \
  "v_dot2_f32_bf16 %3, v87, %8, %3\n\t" \
  "s_nop 2\n\t" \
  : "=&v"(a0), "=&v"(a1), "=&v"(a2), "=&v"(a3) : "v"(y0), "v"(y1), "v"(y2), "v"(y3), "v"(y4) : "memory")
#define QK() asm volatile( \
  "s_waitcnt vmcnt(30)\n\t" \
  "v_dot2_f32_bf16 %0, v128, %8, 0\n\t" \
  "v_dot2_f32_bf16 %0, v129, %9, %0\n\t" \
  "v_dot2_f32_bf16 %0, v130, %10, %0\n\t" \
  "v_dot2_f32_bf16 %0, v131, %11, %0\n\t" \
  "v_dot2_f32_bf16 %0, v132, %12, %0\n\t" \
  "v_dot2_f32_bf16 %0, v133, %13, %0\n\t" \
  "v_dot2_f32_bf16 %0, v134, %14, %0\n\t" \
  "v_dot2_f32_bf16 %0, v135, %15, %0\n\t" \
  "s_waitcnt vmcnt(28)\n\t" \
  "v_dot2_f32_bf16 %1, v136, %8, 0\n\t" \
  "v_dot2_f32_bf16 %1, v137, %9, %1\n\t" \
  "v_dot2_f32_bf16 %1, v138, %10, %1\n\t" \
  "v_dot2_f32_bf16 %1, v139, %11, %1\n\t" \
  "v_dot2_f32_bf16 %1, v140, %12, %1\n\t" \
  "v_dot2_f32_bf16 %1, v141, %13, %1\n\t" \
  "v_dot2_f32_bf16 %1, v142, %14, %1\n\t" \
  "v_dot2_f32_bf16 %1, v143, %15, %1\n\t" \
  "s_waitcnt vmcnt(26)\n\t" \
  "v_dot2_f32_bf16 %2, v144, %8, 0\n\t" \
  "v_dot2_f32_bf16 %2, v145, %9, %2\n\t" \
  "v_dot2_f32_bf16 %2, v146, %10, %2\n\t" \
  "v_dot2_f32_bf16 %2, v147, %11, %2\n\t" \
  "v_dot2_f32_bf16 %2, v148, %12, %2\n\t" \
  "v_dot2_f32_bf16 %2, v149, %13, %2\n\t" \
  "v_dot2_f32_bf16 %2, v150, %14, %2\n\t" \
  "v_dot2_f32_bf16 %2, v151, %15, %2\n\t" \
  "s_waitcnt vmcnt(24)\n\t" \
  "v_dot2_f32_bf16 %3, v152, %8, 0\n\t" \
  "v_dot2_f32_bf16 %3, v153, %9, %3\n\t" \
  "v_dot2_f32_bf16 %3, v154, %10, %3\n\t" \
  "v_dot2_f32_bf16 %3, v155, %11, %3\n\t" \
  "v_dot2_f32_bf16 %3, v156, %12, %3\n\t" \
  "v_dot2_f32_bf16 %3, v157, %13, %3\n\t" \
  "v_dot2_f32_bf16 %3, v158, %14, %3\n\t" \
  "v_dot2_f32_bf16 %3, v159, %15, %3\n\t" \
  "s_waitcnt vmcnt(22)\n\t" \
  "v_dot2_f32_bf16 %4, v160, %8, 0\n\t" \
  "v_dot2_f32_bf16 %4, v161, %9, %4\n\t" \
  "v_dot2_f32_bf16 %4, v162, %10, %4\n\t" \
  "v_dot2_f32_bf16 %4, v163, %11, %4\n\t" \
  "v_dot2_f32_bf16 %4, v164, %12, %4\n\t" \
  "v_dot2_f32_bf16 %4, v165, %13, %4\n\t" \
  "v_dot2_f32_bf16 %4, v166, %14, %4\n\t" \
  "v_dot2_f32_bf16 %4, v167, %15, %4\n\t" \
  "s_waitcnt vmcnt(20)\n\t" \
  "v_dot2_f32_bf16 %5, v168, %8, 0\n\t" \
  "v_dot2_f32_bf16 %5, v169, %9, %5\n\t" \
  "v_dot2_f32_bf16 %5, v170, %10, %5\n\t" \
  "v_dot2_f32_bf16 %5, v171, %11, %5\n\t" \
  "v_dot2_f32_bf16 %5, v172, %12, %5\n\t" \
  "v_dot2_f32_bf16 %5, v173, %13, %5\n\t" \
  "v_dot2_f32_bf16 %5, v174, %14, %5\n\t" \
  "v_dot2_f32_bf16 %5, v175, %15, %5\n\t" \
  "s_waitcnt vmcnt(18)\n\t" \
  "v_dot2_f32_bf16 %6, v176, %8, 0\n\t" \
  "v_dot2_f32_bf16 %6, v177, %9, %6\n\t" \
  "v_dot2_f32_bf16 %6, v178, %10, %6\n\t" \
  "v_dot2_f32_bf16 %6, v179, %11, %6\n\t" \
  "v_dot2_f32_bf16 %6, v180, %12, %6\n\t" \
  "v_dot2_f32_bf16 %6, v181, %13, %6\n\t" \
  "v_dot2_f32_bf16 %6, v182, %14, %6\n\t" \
  "v_dot2_f32_bf16 %6, v183, %15, %6\n\t" \
  "s_waitcnt vmcnt(16)\n\t" \
  "v_dot2_f32_bf16 %7, v184, %8, 0\n\t" \
  "v_dot2_f32_bf16 %7, v185, %9, %7\n\t" \
  "v_dot2_f32_bf16 %7, v186, %10, %7\n\t" \
  "v_dot2_f32_bf16 %7, v187, %11, %7\n\t" \
  "v_dot2_f32_bf16 %7, v188, %12, %7\n\t" \
  "v_dot2_f32_bf16 %7, v189, %13, %7\n\t" \
  "v_dot2_f32_bf16 %7, v190, %14, %7\n\t" \
  "v_dot2_f32_bf16 %7, v191, %15, %7\n\t" \
  "s_nop 2\n\t" \
  "v_add_f32_dpp %0, %0, %0 row_half_mirror row_mask:0xf bank_mask:0x5\n\t" \
  "v_add_f32_dpp %1, %1, %1 row_half_mirror row_mask:0xf bank_mask:0x5\n\t" \
  "v_add_f32_dpp %2, %2, %2 row_half_mirror row_mask:0xf bank_mask:0x5\n\t" \
  "v_add_f32_dpp %3, %3, %3 row_half_mirror row_mask:0xf bank_mask:0x5\n\t" \
  "v_add_f32_dpp %0, %4, %4 row_half_mirror row_mask:0xf bank_mask:0xa\n\t" \
  "v_add_f32_dpp %1, %5, %5 row_half_mirror row_mask:0xf bank_mask:0xa\n\t" \
  "v_add_f32_dpp %2, %6, %6 row_half_mirror row_mask:0xf bank_mask:0xa\n\t" \
  "v_add_f32_dpp %3, %7, %7 row_half_mirror row_mask:0xf bank_mask:0xa\n\t" \
  "s_nop 1\n\t" \
  : "=&v"(a0), "=&v"(a1), "=&v"(a2), "=&v"(a3), "=&v"(a4), "=&v"(a5), "=&v"(a6), "=&v"(a7) : "v"(q0), "v"(q1), "v"(q2), "v"(q3), "v"(q4), "v"(q5), "v"(q6), "v"(q7) : "memory")
#define PVALL(o0, o1, X, L) asm volatile( \
  "v_mov_b32_dpp v124, %2 row_ror:2 row_mask:0xf bank_mask:0x1\n\t" \
  "v_mov_b32_dpp v125, %2 row_ror:6 row_mask:0xf bank_mask:0x2\n\t" \
  "v_mov_b32_dpp v126, %2 row_ror:10 row_mask:0xf bank_mask:0x4\n\t" \
  "v_mov_b32_dpp v127, %2 row_ror:14 row_mask:0xf bank_mask:0x8\n\t" \
  "v_mov_b32_dpp v124, %2 row_ror:2 row_mask:0xf bank_mask:0x1\n\t" \
  "v_mov_b32_dpp v125, %2 row_ror:6 row_mask:0xf bank_mask:0x2\n\t" \
  "v_mfma_f32_16x16x32_bf16 v[56:59], v[120:123], v[240:243], v[56:59]\n\t" \
  "v_mov_b32_dpp v126, %2 row_ror:10 row_mask:0xf bank_mask:0x4\n\t" \
  "v_mov_b32_dpp v127, %2 row_ror:14 row_mask:0xf bank_mask:0x8\n\t" \
  "v_mfma_f32_16x16x32_bf16 v[60:63], v[120:123], v[244:247], v[60:63]\n\t" \
  "s_cbranch_scc1 2f\n\t" \
  "s_waitcnt vmcnt(0)\n\t" \
  "v_perm_b32 v64, v252, v248, %4\n\t" \
  "v_perm_b32 v252, v252, v248, %5\n\t" \
  "v_perm_b32 v65, v253, v249, %4\n\t" \
  "v_perm_b32 v253, v253, v249, %5\n\t" \
  "v_perm_b32 v66, v254, v250, %4\n\t" \
  "v_perm_b32 v254, v254, v250, %5\n\t" \
  "v_perm_b32 v67, v255, v251, %4\n\t" \
  "v_perm_b32 v255, v255, v251, %5\n\t" \
  "v_mov_b32 v248, v64\n\t" \
  "v_mov_b32 v249, v65\n\t" \
  "v_mov_b32 v250, v66\n\t" \
  "v_mov_b32 v251, v67\n\t" \
  "2:\n\t" \
  "s_nop 1\n\t" \
  "v_mfma_f32_16x16x32_bf16 v[56:59], v[124:127], v[248:251], v[56:59]\n\t" \
  "v_mfma_f32_16x16x32_bf16 v[60:63], v[124:127], v[252:255], v[60:63]\n\t" \
  "s_nop 7\n\t" \
  "v_add_f32 %0, v56, v57\n\t" \
  "v_add_f32 %1, v60, v61\n\t" \
  : "=&v"(o0), "=&v"(o1) : "v"(X), "s"(L), "s"(0x05040100u), "s"(0x07060302u) : "memory", "scc", "v56", "v57", "v58", "v59", "v60", "v61", "v62", "v63")
#define AZERO() asm volatile( \
  "v_mov_b32 v120, 0\n\t" \
  "v_mov_b32 v121, 0\n\t" \
  "v_mov_b32 v122, 0\n\t" \
  "v_mov_b32 v123, 0\n\t" \
  "v_mov_b32 v124, 0\n\t" \
  "v_mov_b32 v125, 0\n\t" \
  "v_mov_b32 v126, 0\n\t" \
  "v_mov_b32 v127, 0\n\t" \
  ::: "memory")
#define RWLOAD_W(vo, base) asm volatile( \
  "global_load_dwordx4 v[88:91], %0, %1 offset:0\n\t" \
  "global_load_dwordx4 v[92:95], %0, %1 offset:1280\n\t" \
  "global_load_dwordx4 v[96:99], %0, %1 offset:2560\n\t" \
  "global_load_dwordx4 v[100:103], %0, %1 offset:3840\n\t" \
  : "+v"(vo) : "s"(base) : "memory", "v255")
#define RWLOAD_M1A(vo, base) asm volatile( \
  "global_load_dwordx4 v[88:91], %0, %1 offset:0\n\t" \
  "global_load_dwordx4 v[92:95], %0, %1 offset:3456\n\t" \
  "v_add_u32 %0, 0x1b00, %0\n\t" \
  "global_load_dwordx4 v[96:99], %0, %1 offset:0\n\t" \
  "global_load_dwordx4 v[100:103], %0, %1 offset:3456\n\t" \
  "v_add_u32 %0, 0x1b00, %0\n\t" \
  "global_load_dwordx4 v[104:107], %0, %1 offset:0\n\t" \
  "global_load_dwordx4 v[108:111], %0, %1 offset:3456\n\t" \
  "v_add_u32 %0, 0x1b00, %0\n\t" \
  "global_load_dwordx4 v[112:115], %0, %1 offset:0\n\t" \
  "global_load_dwordx4 v[116:119], %0, %1 offset:3456\n\t" \
  : "+v"(vo) : "s"(base) : "memory", "v255")
#define RWLOAD_M1B(vo, base) asm volatile( \
  "global_load_dwordx4 v[80:83], %0, %1\n\t" \
  "global_load_dwordx4 v[84:87], %0, %1 offset:3456\n\t" \
  :: "v"(vo), "s"(base) : "memory", "v255")
#define RWLOAD_M2(vo, base) asm volatile( \
  "global_load_dwordx4 v[88:91], %0, %1 offset:0\n\t" \
  "global_load_dwordx4 v[92:95], %0, %1 offset:1280\n\t" \
  "global_load_dwordx4 v[96:99], %0, %1 offset:2560\n\t" \
  "global_load_dwordx4 v[100:103], %0, %1 offset:3840\n\t" \
  "v_add_u32 %0, 0x1400, %0\n\t" \
  "global_load_dwordx4 v[104:107], %0, %1 offset:0\n\t" \
  "global_load_dwordx4 v[108:111], %0, %1 offset:1280\n\t" \
  "global_load_dwordx4 v[112:115], %0, %1 offset:2560\n\t" \
  "global_load_dwordx4 v[116:119], %0, %1 offset:3840\n\t" \
  : "+v"(vo) : "s"(base) : "memory", "v255")
#define RWPERM4() asm volatile( \
  "s_waitcnt vmcnt(0)\n\t" \
  "v_perm_b32 v64, v92, v88, %0\n\t" \
  "v_perm_b32 v92, v92, v88, %1\n\t" \
  "v_perm_b32 v65, v93, v89, %0\n\t" \
  "v_perm_b32 v93, v93, v89, %1\n\t" \
  "v_perm_b32 v66, v94, v90, %0\n\t" \
  "v_perm_b32 v94, v94, v90, %1\n\t" \
  "v_perm_b32 v67, v95, v91, %0\n\t" \
  "v_perm_b32 v95, v95, v91, %1\n\t" \
  "v_mov_b32 v88, v64\n\t" \
  "v_mov_b32 v89, v65\n\t" \
  "v_mov_b32 v90, v66\n\t" \
  "v_mov_b32 v91, v67\n\t" \
  "v_perm_b32 v64, v100, v96, %0\n\t" \
  "v_perm_b32 v100, v100, v96, %1\n\t" \
  "v_perm_b32 v65, v101, v97, %0\n\t" \
  "v_perm_b32 v101, v101, v97, %1\n\t" \
  "v_perm_b32 v66, v102, v98, %0\n\t" \
  "v_perm_b32 v102, v102, v98, %1\n\t" \
  "v_perm_b32 v67, v103, v99, %0\n\t" \
  "v_perm_b32 v103, v103, v99, %1\n\t" \
  "v_mov_b32 v96, v64\n\t" \
  "v_mov_b32 v97, v65\n\t" \
  "v_mov_b32 v98, v66\n\t" \
  "v_mov_b32 v99, v67\n\t" \
  "v_perm_b32 v64, v108, v104, %0\n\t" \
  "v_perm_b32 v108, v108, v104, %1\n\t" \
  "v_perm_b32 v65, v109, v105, %0\n\t" \
  "v_perm_b32 v109, v109, v105, %1\n\t" \
  "v_perm_b32 v66, v110, v106, %0\n\t" \
  "v_perm_b32 v110, v110, v106, %1\n\t" \
  "v_perm_b32 v67, v111, v107, %0\n\t" \
  "v_perm_b32 v111, v111, v107, %1\n\t" \
  "v_mov_b32 v104, v64\n\t" \
  "v_mov_b32 v105, v65\n\t" \
  "v_mov_b32 v106, v66\n\t" \
  "v_mov_b32 v107, v67\n\t" \
  "v_perm_b32 v64, v116, v112, %0\n\t" \
  "v_perm_b32 v116, v116, v112, %1\n\t" \
  "v_perm_b32 v65, v117, v113, %0\n\t" \
  "v_perm_b32 v117, v117, v113, %1\n\t" \
  "v_perm_b32 v66, v118, v114, %0\n\t" \
  "v_perm_b32 v118, v118, v114, %1\n\t" \
  "v_perm_b32 v67, v119, v115, %0\n\t" \
  "v_perm_b32 v119, v119, v115, %1\n\t" \
  "v_mov_b32 v112, v64\n\t" \
  "v_mov_b32 v113, v65\n\t" \
  "v_mov_b32 v114, v66\n\t" \
  "v_mov_b32 v115, v67\n\t" \
  :: "s"(0x05040100u), "s"(0x07060302u) : "memory")
#define RWPERM_T() asm volatile( \
  "s_waitcnt vmcnt(0)\n\t" \
  "v_perm_b32 v64, v84, v80, %0\n\t" \
  "v_perm_b32 v84, v84, v80, %1\n\t" \
  "v_perm_b32 v65, v85, v81, %0\n\t" \
  "v_perm_b32 v85, v85, v81, %1\n\t" \
  "v_perm_b32 v66, v86, v82, %0\n\t" \
  "v_perm_b32 v86, v86, v82, %1\n\t" \
  "v_perm_b32 v67, v87, v83, %0\n\t" \
  "v_perm_b32 v87, v87, v83, %1\n\t" \
  "v_mov_b32 v80, v64\n\t" \
  "v_mov_b32 v81, v65\n\t" \
  "v_mov_b32 v82, v66\n\t" \
  "v_mov_b32 v83, v67\n\t" \
  :: "s"(0x05040100u), "s"(0x07060302u) : "memory")
#define RGEMV2(y0, y1) asm volatile( \
  "v_dot2_f32_bf16 %0, v88, %8, 0\n\t" \
  "v_dot2_f32_bf16 %1, v92, %8, 0\n\t" \
  "v_dot2_f32_bf16 %2, v89, %8, 0\n\t" \
  "v_dot2_f32_bf16 %3, v93, %8, 0\n\t" \
  "v_dot2_f32_bf16 %4, v90, %8, 0\n\t" \
  "v_dot2_f32_bf16 %5, v94, %8, 0\n\t" \
  "v_dot2_f32_bf16 %6, v91, %8, 0\n\t" \
  "v_dot2_f32_bf16 %7, v95, %8, 0\n\t" \
  "v_dot2_f32_bf16 %0, v96, %9, %0\n\t" \
  "v_dot2_f32_bf16 %1, v100, %9, %1\n\t" \
  "v_dot2_f32_bf16 %2, v97, %9, %2\n\t" \
  "v_dot2_f32_bf16 %3, v101, %9, %3\n\t" \
  "v_dot2_f32_bf16 %4, v98, %9, %4\n\t" \
  "v_dot2_f32_bf16 %5, v102, %9, %5\n\t" \
  "v_dot2_f32_bf16 %6, v99, %9, %6\n\t" \
  "v_dot2_f32_bf16 %7, v103, %9, %7\n\t" \
  "s_nop 2\n\t" \
  : "=&v"(a0), "=&v"(a1), "=&v"(a2), "=&v"(a3), "=&v"(a4), "=&v"(a5), "=&v"(a6), "=&v"(a7) : "v"(y0), "v"(y1) : "memory")
#define RGEMV4(y0, y1, y2, y3) asm volatile( \
  "v_dot2_f32_bf16 %0, v88, %8, 0\n\t" \
  "v_dot2_f32_bf16 %1, v92, %8, 0\n\t" \
  "v_dot2_f32_bf16 %2, v89, %8, 0\n\t" \
  "v_dot2_f32_bf16 %3, v93, %8, 0\n\t" \
  "v_dot2_f32_bf16 %4, v90, %8, 0\n\t" \
  "v_dot2_f32_bf16 %5, v94, %8, 0\n\t" \
  "v_dot2_f32_bf16 %6, v91, %8, 0\n\t" \
  "v_dot2_f32_bf16 %7, v95, %8, 0\n\t" \
  "v_dot2_f32_bf16 %0, v96, %9, %0\n\t" \
  "v_dot2_f32_bf16 %1, v100, %9, %1\n\t" \
  "v_dot2_f32_bf16 %2, v97, %9, %2\n\t" \
  "v_dot2_f32_bf16 %3, v101, %9, %3\n\t" \
  "v_dot2_f32_bf16 %4, v98, %9, %4\n\t" \
  "v_dot2_f32_bf16 %5, v102, %9, %5\n\t" \
  "v_dot2_f32_bf16 %6, v99, %9, %6\n\t" \
  "v_dot2_f32_bf16 %7, v103, %9, %7\n\t" \
  "v_dot2_f32_bf16 %0, v104, %10, %0\n\t" \
  "v_dot2_f32_bf16 %1, v108, %10, %1\n\t" \
  "v_dot2_f32_bf16 %2, v105, %10, %2\n\t" \
  "v_dot2_f32_bf16 %3, v109, %10, %3\n\t" \
  "v_dot2_f32_bf16 %4, v106, %10, %4\n\t" \
  "v_dot2_f32_bf16 %5, v110, %10, %5\n\t" \
  "v_dot2_f32_bf16 %6, v107, %10, %6\n\t" \
  "v_dot2_f32_bf16 %7, v111, %10, %7\n\t" \
  "v_dot2_f32_bf16 %0, v112, %11, %0\n\t" \
  "v_dot2_f32_bf16 %1, v116, %11, %1\n\t" \
  "v_dot2_f32_bf16 %2, v113, %11, %2\n\t" \
  "v_dot2_f32_bf16 %3, v117, %11, %3\n\t" \
  "v_dot2_f32_bf16 %4, v114, %11, %4\n\t" \
  "v_dot2_f32_bf16 %5, v118, %11, %5\n\t" \
  "v_dot2_f32_bf16 %6, v115, %11, %6\n\t" \
  "v_dot2_f32_bf16 %7, v119, %11, %7\n\t" \
  "s_nop 2\n\t" \
  : "=&v"(a0), "=&v"(a1), "=&v"(a2), "=&v"(a3), "=&v"(a4), "=&v"(a5), "=&v"(a6), "=&v"(a7) : "v"(y0), "v"(y1), "v"(y2), "v"(y3) : "memory")
#define RGEMV5(y0, y1, y2, y3, y4) asm volatile( \
  "v_dot2_f32_bf16 %0, v88, %8, 0\n\t" \
  "v_dot2_f32_bf16 %1, v92, %8, 0\n\t" \
  "v_dot2_f32_bf16 %2, v89, %8, 0\n\t" \
  "v_dot2_f32_bf16 %3, v93, %8, 0\n\t" \
  "v_dot2_f32_bf16 %4, v90, %8, 0\n\t" \
  "v_dot2_f32_bf16 %5, v94, %8, 0\n\t" \
  "v_dot2_f32_bf16 %6, v91, %8, 0\n\t" \
  "v_dot2_f32_bf16 %7, v95, %8, 0\n\t" \
  "v_dot2_f32_bf16 %0, v96, %9, %0\n\t" \
  "v_dot2_f32_bf16 %1, v100, %9, %1\n\t" \
  "v_dot2_f32_bf16 %2, v97, %9, %2\n\t" \
  "v_dot2_f32_bf16 %3, v101, %9, %3\n\t" \
  "v_dot2_f32_bf16 %4, v98, %9, %4\n\t" \
  "v_dot2_f32_bf16 %5, v102, %9, %5\n\t" \
  "v_dot2_f32_bf16 %6, v99, %9, %6\n\t" \
  "v_dot2_f32_bf16 %7, v103, %9, %7\n\t" \
  "v_dot2_f32_bf16 %0, v104, %10, %0\n\t" \
  "v_dot2_f32_bf16 %1, v108, %10, %1\n\t" \
  "v_dot2_f32_bf16 %2, v105, %10, %2\n\t" \
  "v_dot2_f32_bf16 %3, v109, %10, %3\n\t" \
  "v_dot2_f32_bf16 %4, v106, %10, %4\n\t" \
  "v_dot2_f32_bf16 %5, v110, %10, %5\n\t" \
  "v_dot2_f32_bf16 %6, v107, %10, %6\n\t" \
  "v_dot2_f32_bf16 %7, v111, %10, %7\n\t" \
  "v_dot2_f32_bf16 %0, v112, %11, %0\n\t" \
  "v_dot2_f32_bf16 %1, v116, %11, %1\n\t" \
  "v_dot2_f32_bf16 %2, v113, %11, %2\n\t" \
  "v_dot2_f32_bf16 %3, v117, %11, %3\n\t" \
  "v_dot2_f32_bf16 %4, v114, %11, %4\n\t" \
  "v_dot2_f32_bf16 %5, v118, %11, %5\n\t" \
  "v_dot2_f32_bf16 %6, v115, %11, %6\n\t" \
  "v_dot2_f32_bf16 %7, v119, %11, %7\n\t" \
  "v_dot2_f32_bf16 %0, v80, %12, %0\n\t" \
  "v_dot2_f32_bf16 %1, v84, %12, %1\n\t" \
  "v_dot2_f32_bf16 %2, v81, %12, %2\n\t" \
  "v_dot2_f32_bf16 %3, v85, %12, %3\n\t" \
  "v_dot2_f32_bf16 %4, v82, %12, %4\n\t" \
  "v_dot2_f32_bf16 %5, v86, %12, %5\n\t" \
  "v_dot2_f32_bf16 %6, v83, %12, %6\n\t" \
  "v_dot2_f32_bf16 %7, v87, %12, %7\n\t" \
  "s_nop 2\n\t" \
  : "=&v"(a0), "=&v"(a1), "=&v"(a2), "=&v"(a3), "=&v"(a4), "=&v"(a5), "=&v"(a6), "=&v"(a7) : "v"(y0), "v"(y1), "v"(y2), "v"(y3), "v"(y4) : "memory")

#define STAMP(i) do {} while (0)

__global__ void __launch_bounds__(512, 2) __attribute__((amdgpu_num_vgpr(32)))
fused_kernel(const char* __restrict__ xin, const char* __restrict__ Kc, const char* __restrict__ Vc,
             const char* __restrict__ Wq, const char* __restrict__ Wo, const char* __restrict__ W1,
             const char* __restrict__ B1, const char* __restrict__ W2, const char* __restrict__ B2,
             char* __restrict__ out, char* __restrict__ ws) {
  __shared__ Lds S;
  const int tid = threadIdx.x, lane0 = tid & 63;
  const int w = __builtin_amdgcn_readfirstlane(tid >> 6);
  const int b = blockIdx.x, g = b & 7, j = b >> 3, h = 4 * g + (j >> 3), s = j & 7;
  const int m1 = (j - 20) * 8 + g, m2 = g * 5 + (j - 27);
  const int role = j < 20 ? 0 : (j < 27 ? (m1 < 54 ? 1 : 3) : 2);
  const rsrc_t R = __builtin_amdgcn_make_buffer_rsrc(ws, 0, 0x200000, 0x00020000);
  const size_t kvoff = (size_t)(h * 8 + s) * 131072 + (size_t)w * 16384;
  u32 bias = 0;
  u32 x0 = 0, x1 = 0, x2 = 0, x3 = 0, x4 = 0, E = 0;

  {
    const int lane = lane0;
    if (w < 4) {
      const int sg = 16 * w + (lane >> 2);
      u32 o0 = 8 * sg * 8192 + (lane & 3) * 8, o1 = (512 + 2 * sg) * 8192 + (lane & 3) * 8;
      const char* wb = Wq + h * 256 + s * 32;
      WQLOAD(o0, o1, wb);
      const u32 kvo = (32 * ((lane >> 3) & 1) + (lane >> 4)) * 256 + (lane & 7) * 16;
      const char* kb = Kc + kvoff + 4096;
      KLOAD(kvo, kb);
    } else if (w == 4) {
      __builtin_amdgcn_s_setprio(3);
      u32x4 xa = *(const u32x4*)(xin + lane * 16);
      x4 = *(const u32*)(xin + 1024 + lane * 4);
      E = ld32s(R, 0, WS_E);
      x0 = xa.x; x1 = xa.y; x2 = xa.z; x3 = xa.w;
    } else if (w == 7) {
      __builtin_amdgcn_s_setprio(3);
      const u32x4 ff = {SENT, SENT, SENT, SENT};
      u32 vo, so; bool act;
      if (role == 0) { vo = lane * 4; so = O_Z + g * 2560 + j * 128; act = lane < 32; }
      else if (role == 1) { vo = lane * 4; so = O_H + m1 * 128; act = lane < 16; }
      else { vo = (lane >> 3) * 8192 + (lane & 7) * 4; so = O_X + m2 * 128; act = role == 2; }
      if (lane < 33) {
        st128s(ff, R, lane * 16, WS_LB + O_REC + (h * 8 + s) * 640);
        st128s(ff, R, lane * 16, WS_LB + WS_LSTRIDE + O_REC + (h * 8 + s) * 640);
      }
      if (act) {
        st32s(SENT, R, vo, WS_LB + so);
        st32s(SENT, R, vo, WS_LB + WS_LSTRIDE + so);
      }
      __builtin_amdgcn_s_setprio(0);
    } else if (w == 5) {
      if (lane < 16) ((u32*)S.mw)[lane] = SENT;
      if (lane < 7) (&S.f_y)[lane] = 0;
    }
    AZERO();
    asm volatile("s_waitcnt lgkmcnt(0)\n\ts_barrier" ::: "memory");
    if (w == 7) {
      asm volatile("s_waitcnt vmcnt(0)" ::: "memory");
      lds_wait(&S.f_y, 1);
      if (lane == 0) st32s(S.tag, R, 0, WS_FLAGS + b * 4);
    }
  }

#pragma unroll 1
  for (int L = 0; L < 2; L++) {
    const u32 lb = WS_LB + L * WS_LSTRIDE;
    if (w == 4) {
      int lane = lane0; OPAQUE(lane);
      if (L == 1) {
        const u32 xo = WS_LB + O_X + g * 8192;
        int sp = 0;
        while (true) {
          CBAR();
          u32x4 xa = ld128s(R, (lane >> 1) * 128 + (lane & 1) * 16, xo);
          x4 = ld32s(R, (32 + (lane >> 3)) * 128 + (lane & 7) * 4, xo);
          x0 = xa.x; x1 = xa.y; x2 = xa.z; x3 = xa.w;
          u32 mx = umax3(umax3(x0, x1, x2), x3, x4);
          if (__builtin_amdgcn_ballot_w64(mx == SENT) == 0 || ++sp > SPIN_G) break;
        }
      }
      STAMP(1);
      *(u32x4*)&S.xs[4 * lane] = u32x4{x0, x1, x2, x3};
      S.xs[256 + lane] = x4;
      LN_PASS(x0, x1, x2, x3, x4);
      *(u32x4*)&S.ys[4 * lane] = u32x4{x0, x1, x2, x3};
      S.ys[256 + lane] = x4;
      const u32 tag = E + 0x7F4A7C15u;
      S.tag = tag;
      CBAR();
      *(volatile u32*)&S.f_y = L + 1;
      CBAR();
      STAMP(2);
      u32x2 q;
      int sp = 0;
      while (true) {
        CBAR();
        q = ld64s(R, (lane >> 3) * 128 + (lane & 7) * 8, lb + O_Q + h * 1024);
        if (__builtin_amdgcn_ballot_w64(q.y != tag) == 0 || ++sp > SPIN_G) break;
      }
      S.qs[lane] = q.x;
      CBAR();
      *(volatile u32*)&S.f_q = L + 1;
      CBAR();
      __builtin_amdgcn_s_setprio(0);
      STAMP(4);
    } else if (w < 4) {
      int lane = lane0; OPAQUE(lane);
      if (L == 0) WQPERM();
      const int sg = 16 * w + (lane >> 2);
      lds_wait(&S.f_y, L + 1);
      u32x4 y4 = *(const u32x4*)&S.ys[4 * sg];
      u32 y0 = y4.x, y1 = y4.y, y2 = y4.z, y3 = y4.w, y5 = S.ys[256 + sg];
      float a0, a1, a2, a3;
      QGEMV(a0, a1, a2, a3, y0, y1, y2, y3, y5);
      float t = red16(red32(a0, a2), red32(a1, a3));
      t += dppf<ROR4>(t);
      t += dppf<ROR8>(t);
      if ((lane & 12) == 0) S.qpart[w][4 * (lane & 3) + (lane >> 4)] = t;
      u32 c = lds_inc(&S.c_q);
      if (c == 4 * L + 3) {
        if (lane < 8) {
          f32x2 q0 = *(const f32x2*)&S.qpart[0][2 * lane], q1 = *(const f32x2*)&S.qpart[1][2 * lane];
          f32x2 q2 = *(const f32x2*)&S.qpart[2][2 * lane], q3 = *(const f32x2*)&S.qpart[3][2 * lane];
          f32x2 q = (q0 + q1) + (q2 + q3);
          st64s(u32x2{pk(q.x, q.y), S.tag}, R, lane * 8, lb + O_Q + (h * 8 + s) * 128);
        }
        STAMP(3);
      }
    }
    int lane = lane0; OPAQUE(lane);
    if (L == 0) {
      if (w >= 4) {
        const u32 kvo = (32 * ((lane >> 3) & 1) + (lane >> 4)) * 256 + (lane & 7) * 16;
        const char* kb = Kc + kvoff + 4096;
        KLOAD(kvo, kb);
      }
      const u32 vvo = lane * 16;
      const char* vb0 = Vc + kvoff + 4096;
      const char* vb1 = Vc + kvoff + 12288;
      VLOAD(vvo, vb0, vb1);
    }
    lds_wait(&S.f_q, L + 1);
    STAMP(5);
    float sc;
    {
      const int ch = lane & 7;
      u32x4 qa = *(const u32x4*)&S.qs[4 * ch], qb = *(const u32x4*)&S.qs[32 + 4 * ch];
      u32 q0 = qa.x, q1 = qa.y, q2 = qa.z, q3 = qa.w, q4 = qb.x, q5 = qb.y, q6 = qb.z, q7 = qb.w;
      float a0, a1, a2, a3, a4, a5, a6, a7;
      QK();
      const bool b1 = lane & 2, b0 = lane & 1;
      float t0 = (b1 ? a2 : a0) + dppf<QP_X2>(b1 ? a0 : a2);
      float t1 = (b1 ? a3 : a1) + dppf<QP_X2>(b1 ? a1 : a3);
      sc = (b0 ? t1 : t0) + dppf<QP_X1>(b0 ? t0 : t1);
      sc = rb(rb(sc) * 0.08838834764831845f);
    }
    float mx = fmaxf(sc, dppf<QP_X1>(sc));
    mx = fmaxf(mx, dppf<QP_X2>(mx));
    mx = fmaxf(mx, dppf<ROR4>(mx));
    mx = fmaxf(mx, dppf<ROR8>(mx));
    const float mwv = fmaxf(fmaxf(rdlane(mx, 0), rdlane(mx, 16)), fmaxf(rdlane(mx, 32), rdlane(mx, 48)));
    if (lane == 0) *(volatile u32*)&S.mw[L][w] = clampS(f2u(mwv));
    const float p = __builtin_amdgcn_exp2f((sc - mwv) * LOG2E);
    u32 X;
    {
      const bool odd = lane & 1;
      float lo = p - hi2f(f2u(p));
      float own = odd ? lo : p, snd = odd ? p : lo;
      u32 rcv = dppu<QP_X1>(f2u(snd));
      X = __builtin_amdgcn_perm(rcv, f2u(own), odd ? 0x03020706u : 0x07060302u);
    }
    float ls = p + dppf<QP_X1>(p);
    ls += dppf<QP_X2>(ls);
    ls += dppf<ROR4>(ls);
    ls += dppf<ROR8>(ls);
    const float lw = (rdlane(ls, 0) + rdlane(ls, 16)) + (rdlane(ls, 32) + rdlane(ls, 48));
    STAMP(6);
    float oe, oo;
    PVALL(oe, oo, X, L);
    STAMP(7);
    {
      float M;
      int sp = 0;
      while (true) {
        CBAR();
        u32x4 m0 = *(volatile u32x4*)&S.mw[L][0], m1v = *(volatile u32x4*)&S.mw[L][4];
        u32 um = umax3(umax3(m0.x, m0.y, m0.z), umax3(m0.w, m1v.x, m1v.y), umax3(m1v.z, m1v.w, 0u));
        M = fmaxf(fmaxf(fmaxf(u2f(m0.x), u2f(m0.y)), fmaxf(u2f(m0.z), u2f(m0.w))), fmaxf(fmaxf(u2f(m1v.x), u2f(m1v.y)), fmaxf(u2f(m1v.z), u2f(m1v.w))));
        if (__builtin_amdgcn_readfirstlane(um) != SENT || ++sp > SPIN_L) break;
      }
      const float f = __builtin_amdgcn_exp2f((mwv - M) * LOG2E);
      f32x2 o = {oe * f, oo * f};
      *(f32x2*)&S.part[w][8 * (lane & 15) + 2 * (lane >> 4)] = o;
      if (lane == 0) *(f32x4*)&S.part[w][128] = f32x4{lw * f, w == 0 ? M : 0.f, 0.f, 0.f};
      u32 c = lds_inc(&S.c_part);
      if (c == 8 * L + 7) {
        if (lane < 33) {
          const f32x4* pr = (const f32x4*)&S.part[0][4 * lane];
          f32x4 t = ((pr[0] + pr[33]) + (pr[66] + pr[99])) + ((pr[132] + pr[165]) + (pr[198] + pr[231]));
          st128s(u32x4{clampS(f2u(t.x)), clampS(f2u(t.y)), clampS(f2u(t.z)), clampS(f2u(t.w))}, R, lane * 16, lb + O_REC + (h * 8 + s) * 640);
        }
        STAMP(8);
      }
    }
    if (L == 0) {
      if (role == 0) {
        u32 vo = (64 * w + 16 * (lane >> 4) + 4 * (lane & 3)) * 1280 + ((lane >> 2) & 3) * 16;
        const char* base = Wo + (size_t)(512 * g) * 1280 + j * 64;
        RWLOAD_W(vo, base);
      } else if (role == 1) {
        if (w >= 4) {
          const int sg = 16 * (w - 4) + (lane >> 2);
          u32 vo = 8 * sg * 3456 + (lane & 3) * 16, vo2 = (512 + 2 * sg) * 3456 + (lane & 3) * 16;
          const char* base = W1 + m1 * 64;
          RWLOAD_M1A(vo, base);
          RWLOAD_M1B(vo2, base);
          bias = *(const u32*)(B1 + m1 * 64 + (lane & 15) * 4);
          RWPERM_T();
        }
      } else if (role == 2) {
        const int rg = (lane >> 1) < 27 ? (lane >> 1) : 26;
        u32 vo = (216 * w + 8 * rg) * 1280 + (lane & 1) * 16;
        const char* base = W2 + m2 * 32;
        RWLOAD_M2(vo, base);
        bias = *(const u32*)(B2 + m2 * 32 + (lane & 7) * 4);
      }
      if (w == 1) {
        const u32 tag = S.tag;
        int sp = 0;
        while (true) {
          CBAR();
          u32x4 f = ld128s(R, lane * 16, WS_FLAGS);
          bool ok = f.x == tag && f.y == tag && f.z == tag && f.w == tag;
          if (__builtin_amdgcn_ballot_w64(!ok) == 0 || ++sp > SPIN_G) break;
        }
        *(volatile u32*)&S.f_ep = 1;
        if (b == 0 && lane == 0) st32s(tag, R, 0, WS_E);
      }
      RWPERM4();
      lds_wait(&S.f_ep, 1);
    }
    STAMP(9);
    float a0, a1, a2, a3, a4, a5, a6, a7;
    if (role == 0) {
      const int c = lane >> 4, sq = (lane >> 2) & 3, e = lane & 3;
      const u32 rb0 = lb + O_REC + (4 * g + (w >> 1)) * 8 * 640;
      const u32 va = sq * 640 + (w & 1) * 256 + (4 * c + e) * 16, vm = (sq + 4 * (e & 1)) * 640 + 512;
      u32x4 A, Bv; u32x2 ML;
      int sp = 0;
      while (true) {
        CBAR();
        A = ld128s(R, va, rb0);
        Bv = ld128s(R, va + 2560, rb0);
        ML = ld64s(R, vm, rb0);
        u32 mxu = umax3(umax3(A.x, A.y, A.z), umax3(A.w, Bv.x, Bv.y), umax3(umax3(Bv.z, Bv.w, ML.x), ML.y, 0u));
        if (__builtin_amdgcn_ballot_w64(mxu == SENT) == 0 || ++sp > SPIN_G) break;
      }
      STAMP(10);
      const float Mr = u2f(ML.y);
      float Mx = fmaxf(Mr, dppf<QP_X1>(Mr));
      Mx = fmaxf(Mx, dppf<ROR4>(Mx));
      Mx = fmaxf(Mx, dppf<ROR8>(Mx));
      const float f = __builtin_amdgcn_exp2f((Mr - Mx) * LOG2E);
      float Ls = u2f(ML.x) * f;
      Ls += dppf<QP_X1>(Ls);
      Ls += dppf<ROR4>(Ls);
      Ls += dppf<ROR8>(Ls);
      const float ri = __builtin_amdgcn_rcpf(Ls);
      const float fa = dppf<QP_B0>(f), fb = dppf<QP_B1>(f);
      float o0 = u2f(A.x) * fa + u2f(Bv.x) * fb, o1 = u2f(A.y) * fa + u2f(Bv.y) * fb;
      float o2 = u2f(A.z) * fa + u2f(Bv.z) * fb, o3 = u2f(A.w) * fa + u2f(Bv.w) * fb;
      o0 += dppf<ROR4>(o0); o1 += dppf<ROR4>(o1); o2 += dppf<ROR4>(o2); o3 += dppf<ROR4>(o3);
      o0 += dppf<ROR8>(o0); o1 += dppf<ROR8>(o1); o2 += dppf<ROR8>(o2); o3 += dppf<ROR8>(o3);
      const u32 p0 = pk(o0 * ri, o1 * ri), p1 = pk(o2 * ri, o3 * ri);
      RGEMV2(p0, p1);
      float t0 = red16(red32(a0, a4), red32(a2, a6)), t1 = red16(red32(a1, a5), red32(a3, a7));
      t0 += dppf<QP_X1>(t0); t1 += dppf<QP_X1>(t1);
      t0 += dppf<QP_X2>(t0); t1 += dppf<QP_X2>(t1);
      if (e == 0) *(f32x2*)&S.wpart[w][8 * sq + 2 * c] = f32x2{t0, t1};
      u32 cc = lds_inc(&S.c_role);
      if (cc == 8 * L + 7) {
        if (lane < 32) {
          const float* r = &S.wpart[0][lane];
          float t = ((r[0] + r[32]) + (r[64] + r[96])) + ((r[128] + r[160]) + (r[192] + r[224]));
          st32s(clampS(f2u(t)), R, lane * 4, lb + O_Z + g * 2560 + j * 128);
        }
        STAMP(11);
      }
    } else if (role == 1) {
      if (w < 5) {
        const int hb = lane >> 5, i = lane & 31;
        const u32 xp = S.xs[64 * w + 2 * i + hb];
        const u32 va = hb * 2560 + w * 512 + i * 16;
        u32x4 z0, z1, z2, z3;
        int sp = 0;
        while (true) {
          CBAR();
          z0 = ld128s(R, va, lb + O_Z); z1 = ld128s(R, va, lb + O_Z + 5120);
          z2 = ld128s(R, va, lb + O_Z + 10240); z3 = ld128s(R, va, lb + O_Z + 15360);
          u32 mxu = umax3(umax3(umax3(z0.x, z0.y, z0.z), umax3(z0.w, z1.x, z1.y), umax3(z1.z, z1.w, z2.x)),
                          umax3(umax3(z2.y, z2.z, z2.w), umax3(z3.x, z3.y, z3.z), z3.w), 0u);
          if (__builtin_amdgcn_ballot_w64(mxu == SENT) == 0 || ++sp > SPIN_G) break;
        }
        STAMP(10);
        f32x4 zs = (__builtin_bit_cast(f32x4, z0) + __builtin_bit_cast(f32x4, z1)) + (__builtin_bit_cast(f32x4, z2) + __builtin_bit_cast(f32x4, z3));
        float za = red32(zs.x, zs.z), zb = red32(zs.y, zs.w);
        S.x1s[64 * w + 2 * i + hb] = pk(lo2f(xp) + rb(za), hi2f(xp) + rb(zb));
        lds_inc(&S.c_x1);
      }
      if (w >= 4) {
        const int sg = 16 * (((lane & 3) + w) & 3) + (lane >> 2);
        lds_wait(&S.c_x1, 5 * (L + 1));
        u32x4 xv = *(const u32x4*)&S.x1s[4 * sg];
        u32 p0 = xv.x, p1 = xv.y, p2 = xv.z, p3 = xv.w, p4 = S.x1s[256 + sg];
#pragma unroll 1
        for (int it = 0; it < 2; it++) LN_PASS(p0, p1, p2, p3, p4);
        p0 = dppu<QP_B0>(p0); p1 = dppu<QP_B0>(p1); p2 = dppu<QP_B0>(p2); p3 = dppu<QP_B0>(p3); p4 = dppu<QP_B0>(p4);
        RGEMV5(p0, p1, p2, p3, p4);
        float t0 = red16(red32(a0, a4), red32(a2, a6)), t1 = red16(red32(a1, a5), red32(a3, a7));
        t0 += dppf<ROR4>(t0); t1 += dppf<ROR4>(t1);
        t0 += dppf<ROR8>(t0); t1 += dppf<ROR8>(t1);
        if ((lane & 12) == 0) *(f32x2*)&S.wpart[w - 4][8 * (lane & 3) + 2 * (lane >> 4)] = f32x2{t0, t1};
        u32 cc = lds_inc(&S.c_role);
        if (cc == 4 * L + 3) {
          if (lane < 16) {
            const f32x2* r = (const f32x2*)&S.wpart[0][2 * lane];
            f32x2 t = (r[0] + r[16]) + (r[32] + r[48]);
            float g0 = rb(rb(t.x) + lo2f(bias)), g1 = rb(rb(t.y) + hi2f(bias));
            const float C1 = -2.f * 0.7978845608028654f * LOG2E, C3 = C1 * 0.044715f;
            float e0 = __builtin_amdgcn_exp2f(g0 * __builtin_fmaf(C3 * g0, g0, C1));
            float e1 = __builtin_amdgcn_exp2f(g1 * __builtin_fmaf(C3 * g1, g1, C1));
            float h0 = g0 * __builtin_amdgcn_rcpf(1.f + e0), h1 = g1 * __builtin_amdgcn_rcpf(1.f + e1);
            st32s(clampS(pk(h0, h1)), R, lane * 4, lb + O_H + m1 * 128);
          }
          STAMP(11);
        }
      }
    } else if (role == 2) {
      if (w == 0) {
        const u32 xp = S.xs[8 * m2 + (lane & 7)];
        u32x2 z;
        int sp = 0;
        while (true) {
          CBAR();
          z = ld64s(R, (lane >> 3) * 2560 + (lane & 7) * 8, lb + O_Z + m2 * 64);
          if (__builtin_amdgcn_ballot_w64(umax3(z.x, z.y, 0u) == SENT) == 0 || ++sp > SPIN_G) break;
        }
        float za = u2f(z.x), zb = u2f(z.y);
        za = red16(za, za); zb = red16(zb, zb);
        za = red32(za, za); zb = red32(zb, zb);
        za += dppf<ROR8>(za); zb += dppf<ROR8>(zb);
        if (lane < 8) S.x1m[lane] = pk(lo2f(xp) + rb(za), hi2f(xp) + rb(zb));
      }
      const int rg = (lane >> 1) < 27 ? (lane >> 1) : 26;
      const int kk = 27 * w + rg;
      const u32 va = (kk >> 2) * 128 + (kk & 3) * 16;
      u32x4 hv;
      int sp = 0;
      while (true) {
        CBAR();
        hv = ld128s(R, va, lb + O_H);
        if (__builtin_amdgcn_ballot_w64(umax3(umax3(hv.x, hv.y, hv.z), hv.w, 0u) == SENT) == 0 || ++sp > SPIN_G) break;
      }
      STAMP(10);
      if (lane >= 54) hv = u32x4{0, 0, 0, 0};
      const u32 p0 = hv.x, p1 = hv.y, p2 = hv.z, p3 = hv.w;
      RGEMV4(p0, p1, p2, p3);
      float t0 = red16(red32(a0, a4), red32(a2, a6)), t1 = red16(red32(a1, a5), red32(a3, a7));
      t0 += dppf<QP_X2>(t0); t1 += dppf<QP_X2>(t1);
      t0 += dppf<ROR4>(t0); t1 += dppf<ROR4>(t1);
      t0 += dppf<ROR8>(t0); t1 += dppf<ROR8>(t1);
      if ((lane & 14) == 0) *(f32x2*)&S.wpart[w][8 * (lane & 1) + 2 * (lane >> 4)] = f32x2{t0, t1};
      u32 cc = lds_inc(&S.c_role);
      if (cc == 8 * L + 7) {
        const int d = lane & 7;
        const f32x2* r = (const f32x2*)&S.wpart[0][2 * d];
        const u32 xr = S.x1m[d];
        f32x2 t = ((r[0] + r[16]) + (r[32] + r[48])) + ((r[64] + r[80]) + (r[96] + r[112]));
        const u32 o = pk(lo2f(xr) + rb(rb(t.x) + lo2f(bias)), hi2f(xr) + rb(rb(t.y) + hi2f(bias)));
        if (L == 0) st32s(clampS(o), R, (lane >> 3) * 8192 + d * 4, lb + O_X + m2 * 128);
        else if (lane < 8) *(u32*)(out + m2 * 32 + d * 4) = o;
        STAMP(11);
      }
    }
  }
}

extern "C" void kernel_launch(void* const* d_in, const int* in_sizes, int n_in, void* d_out, int out_size,
                              void* d_ws, size_t ws_size, hipStream_t stream) {
  fused_kernel<<<256, 512, 0, stream>>>((const char*)d_in[0], (const char*)d_in[1], (const char*)d_in[2],
                                        (const char*)d_in[3], (const char*)d_in[4], (const char*)d_in[6],
                                        (const char*)d_in[7], (const char*)d_in[8], (const char*)d_in[9],
                                        (char*)d_out, (char*)d_ws);
}
```

```cpp
#include <hip/hip_runtime.h>
#include <stdint.h>

typedef unsigned u32;
typedef unsigned long long u64;
typedef u32 u32x4 __attribute__((ext_vector_type(4)));
typedef u32 u32x2 __attribute__((ext_vector_type(2)));
typedef float f32x4 __attribute__((ext_vector_type(4)));
typedef float f32x2 __attribute__((ext_vector_type(2)));
typedef __bf16 bf16x2 __attribute__((ext_vector_type(2)));
typedef __bf16 bf16x8 __attribute__((ext_vector_type(8)));
typedef __amdgpu_buffer_rsrc_t rsrc_t;

#define DEV __device__ __forceinline__
#define CBAR() asm volatile("" ::: "memory")
#define SB() __builtin_amdgcn_sched_barrier(0)

#define DIAG 0

constexpr u32 WS_E = 0;
constexpr u32 WS_FLAGS = 4096;
constexpr u32 WS_LB = 16384;
constexpr u32 WS_LSTRIDE = 0x50000;
constexpr u32 O_REC = 0;
constexpr u32 O_Z = 0x28000;
constexpr u32 O_H = 0x2E000;
constexpr u32 O_X = 0x30000;
constexpr u32 O_Q = 0x40000;
constexpr u32 WS_DIAG = 0x100000;
constexpr u32 SENT = 0xFFFFFFFFu;
constexpr int SPIN_G = 1 << 16;
constexpr int SPIN_L = 1 << 22;
constexpr float LOG2E = 1.4426950408889634f;

DEV float u2f(u32 v) { return __builtin_bit_cast(float, v); }
DEV u32 f2u(float v) { return __builtin_bit_cast(u32, v); }
DEV float dot2(u32 a, u32 b, float c) {
  return __builtin_amdgcn_fdot2_f32_bf16(__builtin_bit_cast(bf16x2, a), __builtin_bit_cast(bf16x2, b), c, false);
}
DEV u32 pk(float lo, float hi) {
  f32x2 t = {lo, hi};
  bf16x2 p = __builtin_convertvector(t, bf16x2);
  return __builtin_bit_cast(u32, p);
}
DEV float rb(float x) { return u2f(pk(0.f, x)); }
DEV float lo2f(u32 p) { return u2f(p << 16); }
DEV float hi2f(u32 p) { return u2f(p & 0xffff0000u); }
template <int CTRL> DEV float dppf(float x) { return u2f((u32)__builtin_amdgcn_mov_dpp((int)f2u(x), CTRL, 0xf, 0xf, true)); }
template <int CTRL> DEV u32 dppu(u32 x) { return (u32)__builtin_amdgcn_mov_dpp((int)x, CTRL, 0xf, 0xf, true); }
constexpr int QP_X1 = 0xB1, QP_X2 = 0x4E, QP_B0 = 0x00, QP_B1 = 0x55, ROR4 = 0x124, ROR8 = 0x128, HMIR = 0x141;
DEV float red32(float a, float b) {
  auto s = __builtin_amdgcn_permlane32_swap(f2u(a), f2u(b), false, false);
  u32 x = s[0], y = s[1];
  return u2f(x) + u2f(y);
}
DEV float red16(float a, float b) {
  auto s = __builtin_amdgcn_permlane16_swap(f2u(a), f2u(b), false, false);
  u32 x = s[0], y = s[1];
  return u2f(x) + u2f(y);
}
DEV float rdlane(float x, int l) { return u2f((u32)__builtin_amdgcn_readlane((int)f2u(x), l)); }
DEV u32 umax3(u32 a, u32 b, u32 c) { u32 m = a > b ? a : b; return m > c ? m : c; }
DEV u32 clampS(u32 x) { return x < 0xFFFFFFFEu ? x : 0xFFFFFFFEu; }
DEV u32 plo(u32 a, u32 b) { return __builtin_amdgcn_perm(b, a, 0x05040100u); }
DEV u32 phi(u32 a, u32 b) { return __builtin_amdgcn_perm(b, a, 0x07060302u); }
DEV u64 rt() { return __builtin_amdgcn_s_memrealtime(); }

DEV u32x4 ld128s(rsrc_t r, u32 v, u32 s) { return __builtin_amdgcn_raw_buffer_load_b128(r, v, s, 16); }
DEV u32x2 ld64s(rsrc_t r, u32 v, u32 s) { return __builtin_amdgcn_raw_buffer_load_b64(r, v, s, 16); }
DEV u32 ld32s(rsrc_t r, u32 v, u32 s) { return __builtin_amdgcn_raw_buffer_load_b32(r, v, s, 16); }
DEV void st128s(u32x4 d, rsrc_t r, u32 v, u32 s) { __builtin_amdgcn_raw_buffer_store_b128(d, r, v, s, 16); }
DEV void st64s(u32x2 d, rsrc_t r, u32 v, u32 s) { __builtin_amdgcn_raw_buffer_store_b64(d, r, v, s, 16); }
DEV void st32s(u32 d, rsrc_t r, u32 v, u32 s) { __builtin_amdgcn_raw_buffer_store_b32(d, r, v, s, 16); }

#define OPAQUE(x) asm volatile("" : "+v"(x))

struct __attribute__((aligned(16))) Lds {
  u32 ys[320];
  u32 xs[320];
  u32 x1s[320];
  u32 qs[64];
  float qpart[4][16];
  float mw[2][8];
  float part[8][132];
  float wpart[8][32];
  u32 x1m[8];
  u32 tag;
  u32 f_y, f_q, f_ep, c_q, c_part, c_role, c_x1;
};

DEV void lds_wait(u32* f, u32 v) {
  int sp = 0;
  while (true) {
    CBAR();
    u32 t = __builtin_amdgcn_readfirstlane(*f);
    if (t >= v || ++sp > SPIN_L) break;
  }
  CBAR();
}
typedef __attribute__((address_space(3))) u32 lds_u32;
DEV u32 lds_inc(u32* c) {
  u32 r;
  const u32 addr = (u32)(size_t)(lds_u32*)c;
  asm volatile("s_mov_b64 exec, 1\n\tds_add_rtn_u32 %0, %1, %2\n\ts_mov_b64 exec, -1\n\ts_waitcnt lgkmcnt(0)"
               : "=&v"(r) : "v"(addr), "v"(1u) : "memory");
  return __builtin_amdgcn_readfirstlane(r);
}
DEV void lds_inc_nr(u32* c) {
  const u32 addr = (u32)(size_t)(lds_u32*)c;
  asm volatile("s_mov_b64 exec, 1\n\tds_add_u32 %0, %1\n\ts_mov_b64 exec, -1" :: "v"(addr), "v"(1u) : "memory");
}
#pragma float_control(precise, off, push)
DEV float fmx(float a, float b) { return __builtin_fmaxf(a, b); }
#pragma float_control(pop)

#define LN_PASS(p0, p1, p2, p3, p4) do { \
    float s_ = dot2(p0, 0x3f803f80u, 0.f), ss_ = dot2(p0, p0, 0.f); \
    s_ = dot2(p1, 0x3f803f80u, s_); ss_ = dot2(p1, p1, ss_); \
    s_ = dot2(p2, 0x3f803f80u, s_); ss_ = dot2(p2, p2, ss_); \
    s_ = dot2(p3, 0x3f803f80u, s_); ss_ = dot2(p3, p3, ss_); \
    s_ = dot2(p4, 0x3f803f80u, s_); ss_ = dot2(p4, p4, ss_); \
    float t_ = red32(s_, ss_); \
    t_ += dppf<QP_X1>(t_); t_ += dppf<QP_X2>(t_); t_ += dppf<ROR4>(t_); t_ += dppf<ROR8>(t_); \
    const float S_ = rdlane(t_, 0) + rdlane(t_, 16), SS_ = rdlane(t_, 32) + rdlane(t_, 48); \
    const float mean_ = S_ * (1.f / 640.f); \
    const float var_ = fmx(__builtin_fmaf(-mean_, mean_, SS_ * (1.f / 640.f)), 0.f); \
    const float m_ = rb(mean_); \
    const float r_ = __builtin_amdgcn_rcpf(rb(__builtin_amdgcn_sqrtf(rb(rb(var_) + 1e-5f)))); \
    p0 = pk(rb(lo2f(p0) - m_) * r_, rb(hi2f(p0) - m_) * r_); \
    p1 = pk(rb(lo2f(p1) - m_) * r_, rb(hi2f(p1) - m_) * r_); \
    p2 = pk(rb(lo2f(p2) - m_) * r_, rb(hi2f(p2) - m_) * r_); \
    p3 = pk(rb(lo2f(p3) - m_) * r_, rb(hi2f(p3) - m_) * r_); \
    p4 = pk(rb(lo2f(p4) - m_) * r_, rb(hi2f(p4) - m_) * r_); \
  } while (0)

#define KLOAD(vo, base) asm volatile( \
  "global_load_dwordx4 v[128:131], %0, %1 offset:-4096 nt\n\t" \
  "global_load_dwordx4 v[132:135], %0, %1 offset:-3968 nt\n\t" \
  "global_load_dwordx4 v[136:139], %0, %1 offset:-3072 nt\n\t" \
  "global_load_dwordx4 v[140:143], %0, %1 offset:-2944 nt\n\t" \
  "global_load_dwordx4 v[144:147], %0, %1 offset:-2048 nt\n\t" \
  "global_load_dwordx4 v[148:151], %0, %1 offset:-1920 nt\n\t" \
  "global_load_dwordx4 v[152:155], %0, %1 offset:-1024 nt\n\t" \
  "global_load_dwordx4 v[156:159], %0, %1 offset:-896 nt\n\t" \
  "global_load_dwordx4 v[160:163], %0, %1 offset:0 nt\n\t" \
  "global_load_dwordx4 v[164:167], %0, %1 offset:128 nt\n\t" \
  "global_load_dwordx4 v[168:171], %0, %1 offset:1024 nt\n\t" \
  "global_load_dwordx4 v[172:175], %0, %1 offset:1152 nt\n\t" \
  "global_load_dwordx4 v[176:179], %0, %1 offset:2048 nt\n\t" \
  "global_load_dwordx4 v[180:183], %0, %1 offset:2176 nt\n\t" \
  "global_load_dwordx4 v[184:187], %0, %1 offset:3072 nt\n\t" \
  "global_load_dwordx4 v[188:191], %0, %1 offset:3200 nt\n\t" \
  :: "v"(vo), "s"(base) : "memory", "v255")
#define VLOAD(vo, b0, b1) asm volatile( \
  "global_load_dwordx4 v[192:195], %0, %1 offset:-4096 nt\n\t" \
  "global_load_dwordx4 v[196:199], %0, %1 offset:-3072 nt\n\t" \
  "global_load_dwordx4 v[200:203], %0, %1 offset:-2048 nt\n\t" \
  "global_load_dwordx4 v[204:207], %0, %1 offset:-1024 nt\n\t" \
  "global_load_dwordx4 v[208:211], %0, %1 offset:0 nt\n\t" \
  "global_load_dwordx4 v[212:215], %0, %1 offset:1024 nt\n\t" \
  "global_load_dwordx4 v[216:219], %0, %1 offset:2048 nt\n\t" \
  "global_load_dwordx4 v[220:223], %0, %1 offset:3072 nt\n\t" \
  "global_load_dwordx4 v[224:227], %0, %2 offset:-4096 nt\n\t" \
  "global_load_dwordx4 v[228:231], %0, %2 offset:-3072 nt\n\t" \
  "global_load_dwordx4 v[232:235], %0, %2 offset:-2048 nt\n\t" \
  "global_load_dwordx4 v[236:239], %0, %2 offset:-1024 nt\n\t" \
  "global_load_dwordx4 v[240:243], %0, %2 offset:0 nt\n\t" \
  "global_load_dwordx4 v[244:247], %0, %2 offset:1024 nt\n\t" \
  "global_load_dwordx4 v[248:251], %0, %2 offset:2048 nt\n\t" \
  "global_load_dwordx4 v[252:255], %0, %2 offset:3072 nt\n\t" \
  :: "v"(vo), "s"(b0), "s"(b1) : "memory", "v255")
#define WQLOAD(o0, o1, base) asm volatile( \
  "global_load_dwordx2 v[192:193], %0, %2\n\t" \
  "v_add_u32 %0, 0x2000, %0\n\t" \
  "global_load_dwordx2 v[194:195], %0, %2\n\t" \
  "v_add_u32 %0, 0x2000, %0\n\t" \
  "global_load_dwordx2 v[196:197], %0, %2\n\t" \
  "v_add_u32 %0, 0x2000, %0\n\t" \
  "global_load_dwordx2 v[198:199], %0, %2\n\t" \
  "v_add_u32 %0, 0x2000, %0\n\t" \
  "global_load_dwordx2 v[200:201], %0, %2\n\t" \
  "v_add_u32 %0, 0x2000, %0\n\t" \
  "global_load_dwordx2 v[202:203], %0, %2\n\t" \
  "v_add_u32 %0, 0x2000, %0\n\t" \
  "global_load_dwordx2 v[204:205], %0, %2\n\t" \
  "v_add_u32 %0, 0x2000, %0\n\t" \
  "global_load_dwordx2 v[206:207], %0, %2\n\t" \
  "global_load_dwordx2 v[208:209], %1, %2\n\t" \
  "v_add_u32 %1, 0x2000, %1\n\t" \
  "global_load_dwordx2 v[210:211], %1, %2\n\t" \
  : "+v"(o0), "+v"(o1) : "s"(base) : "memory", "v255")
#define WQPERM() asm volatile( \
  "s_waitcnt vmcnt(16)\n\t" \
  "v_perm_b32 v68, v194, v192, %0\n\t" \
  "v_perm_b32 v69, v194, v192, %1\n\t" \
  "v_perm_b32 v70, v195, v193, %0\n\t" \
  "v_perm_b32 v71, v195, v193, %1\n\t" \
  "v_perm_b32 v72, v198, v196, %0\n\t" \
  "v_perm_b32 v73, v198, v196, %1\n\t" \
  "v_perm_b32 v74, v199, v197, %0\n\t" \
  "v_perm_b32 v75, v199, v197, %1\n\t" \
  "v_perm_b32 v76, v202, v200, %0\n\t" \
  "v_perm_b32 v77, v202, v200, %1\n\t" \
  "v_perm_b32 v78, v203, v201, %0\n\t" \
  "v_perm_b32 v79, v203, v201, %1\n\t" \
  "v_perm_b32 v80, v206, v204, %0\n\t" \
  "v_perm_b32 v81, v206, v204, %1\n\t" \
  "v_perm_b32 v82, v207, v205, %0\n\t" \
  "v_perm_b32 v83, v207, v205, %1\n\t" \
  "v_perm_b32 v84, v210, v208, %0\n\t" \
  "v_perm_b32 v85, v210, v208, %1\n\t" \
  "v_perm_b32 v86, v211, v209, %0\n\t" \
  "v_perm_b32 v87, v211, v209, %1\n\t" \
  :: "s"(0x05040100u), "s"(0x07060302u) : "memory")
#define QGEMV(a0, a1, a2, a3, y0, y1, y2, y3, y4) asm volatile( \
  "v_dot2_f32_bf16 %0, v68, %4, 0\n\t" \
  "v_dot2_f32_bf16 %1, v69, %4, 0\n\t" \
  "v_dot2_f32_bf16 %2, v70, %4, 0\n\t" \
  "v_dot2_f32_bf16 %3, v71, %4, 0\n\t" \
  "v_dot2_f32_bf16 %0, v72, %5, %0\n\t" \
  "v_dot2_f32_bf16 %1, v73, %5, %1\n\t" \
  "v_dot2_f32_bf16 %2, v74, %5, %2\n\t" \
  "v_dot2_f32_bf16 %3, v75, %5, %3\n\t" \
  "v_dot2_f32_bf16 %0, v76, %6, %0\n\t" \
  "v_dot2_f32_bf16 %1, v77, %6, %1\n\t" \
  "v_dot2_f32_bf16 %2, v78, %6, %2\n\t" \
  "v_dot2_f32_bf16 %3, v79, %6, %3\n\t" \
  "v_dot2_f32_bf16 %0, v80, %7, %0\n\t" \
  "v_dot2_f32_bf16 %1, v81, %7, %1\n\t" \
  "v_dot2_f32_bf16 %2, v82, %7, %2\n\t" \
  "v_dot2_f32_bf16 %3, v83, %7, %3\n\t" \
  "v_dot2_f32_bf16 %0, v84, %8, %0\n\t" \
  "v_dot2_f32_bf16 %1, v85, %8, %1\n\t" \
  "v_dot2_f32_bf16 %2, v86, %8, %2\n\t" \
  "v_dot2_f32_bf16 %3, v87, %8, %3\n\t" \
  "s_nop 2\n\t" \
  : "=&v"(a0), "=&v"(a1), "=&v"(a2), "=&v"(a3) : "v"(y0), "v"(y1), "v"(y2), "v"(y3), "v"(y4) : "memory")
#define QK() asm volatile( \
  "s_waitcnt vmcnt(30)\n\t" \
  "v_dot2_f32_bf16 %0, v128, %8, 0\n\t" \
  "v_dot2_f32_bf16 %0, v129, %9, %0\n\t" \
  "v_dot2_f32_bf16 %0, v130, %10, %0\n\t" \
  "v_dot2_f32_bf16 %0, v131, %11, %0\n\t" \
  "v_dot2_f32_bf16 %0, v132, %12, %0\n\t" \
  "v_dot2_f32_bf16 %0, v133, %13, %0\n\t" \
  "v_dot2_f32_bf16 %0, v134, %14, %0\n\t" \
  "v_dot2_f32_bf16 %0, v135, %15, %0\n\t" \
  "s_waitcnt vmcnt(28)\n\t" \
  "v_dot2_f32_bf16 %1, v136, %8, 0\n\t" \
  "v_dot2_f32_bf16 %1, v137, %9, %1\n\t" \
  "v_dot2_f32_bf16 %1, v138, %10, %1\n\t" \
  "v_dot2_f32_bf16 %1, v139, %11, %1\n\t" \
  "v_dot2_f32_bf16 %1, v140, %12, %1\n\t" \
  "v_dot2_f32_bf16 %1, v141, %13, %1\n\t" \
  "v_dot2_f32_bf16 %1, v142, %14, %1\n\t" \
  "v_dot2_f32_bf16 %1, v143, %15, %1\n\t" \
  "s_waitcnt vmcnt(26)\n\t" \
  "v_dot2_f32_bf16 %2, v144, %8, 0\n\t" \
  "v_dot2_f32_bf16 %2, v145, %9, %2\n\t" \
  "v_dot2_f32_bf16 %2, v146, %10, %2\n\t" \
  "v_dot2_f32_bf16 %2, v147, %11, %2\n\t" \
  "v_dot2_f32_bf16 %2, v148, %12, %2\n\t" \
  "v_dot2_f32_bf16 %2, v149, %13, %2\n\t" \
  "v_dot2_f32_bf16 %2, v150, %14, %2\n\t" \
  "v_dot2_f32_bf16 %2, v151, %15, %2\n\t" \
  "s_waitcnt vmcnt(24)\n\t" \
  "v_dot2_f32_bf16 %3, v152, %8, 0\n\t" \
  "v_dot2_f32_bf16 %3, v153, %9, %3\n\t" \
  "v_dot2_f32_bf16 %3, v154, %10, %3\n\t" \
  "v_dot2_f32_bf16 %3, v155, %11, %3\n\t" \
  "v_dot2_f32_bf16 %3, v156, %12, %3\n\t" \
  "v_dot2_f32_bf16 %3, v157, %13, %3\n\t" \
  "v_dot2_f32_bf16 %3, v158, %14, %3\n\t" \
  "v_dot2_f32_bf16 %3, v159, %15, %3\n\t" \
  "s_waitcnt vmcnt(22)\n\t" \
  "v_dot2_f32_bf16 %4, v160, %8, 0\n\t" \
  "v_dot2_f32_bf16 %4, v161, %9, %4\n\t" \
  "v_dot2_f32_bf16 %4, v162, %10, %4\n\t" \
  "v_dot2_f32_bf16 %4, v163, %11, %4\n\t" \
  "v_dot2_f32_bf16 %4, v164, %12, %4\n\t" \
  "v_dot2_f32_bf16 %4, v165, %13, %4\n\t" \
  "v_dot2_f32_bf16 %4, v166, %14, %4\n\t" \
  "v_dot2_f32_bf16 %4, v167, %15, %4\n\t" \
  "s_waitcnt vmcnt(20)\n\t" \
  "v_dot2_f32_bf16 %5, v168, %8, 0\n\t" \
  "v_dot2_f32_bf16 %5, v169, %9, %5\n\t" \
  "v_dot2_f32_bf16 %5, v170, %10, %5\n\t" \
  "v_dot2_f32_bf16 %5, v171, %11, %5\n\t" \
  "v_dot2_f32_bf16 %5, v172, %12, %5\n\t" \
  "v_dot2_f32_bf16 %5, v173, %13, %5\n\t" \
  "v_dot2_f32_bf16 %5, v174, %14, %5\n\t" \
  "v_dot2_f32_bf16 %5, v175, %15, %5\n\t" \
  "s_waitcnt vmcnt(18)\n\t" \
  "v_dot2_f32_bf16 %6, v176, %8, 0\n\t" \
  "v_dot2_f32_bf16 %6, v177, %9, %6\n\t" \
  "v_dot2_f32_bf16 %6, v178, %10, %6\n\t" \
  "v_dot2_f32_bf16 %6, v179, %11, %6\n\t" \
  "v_dot2_f32_bf16 %6, v180, %12, %6\n\t" \
  "v_dot2_f32_bf16 %6, v181, %13, %6\n\t" \
  "v_dot2_f32_bf16 %6, v182, %14, %6\n\t" \
  "v_dot2_f32_bf16 %6, v183, %15, %6\n\t" \
  "s_waitcnt vmcnt(16)\n\t" \
  "v_dot2_f32_bf16 %7, v184, %8, 0\n\t" \
  "v_dot2_f32_bf16 %7, v185, %9, %7\n\t" \
  "v_dot2_f32_bf16 %7, v186, %10, %7\n\t" \
  "v_dot2_f32_bf16 %7, v187, %11, %7\n\t" \
  "v_dot2_f32_bf16 %7, v188, %12, %7\n\t" \
  "v_dot2_f32_bf16 %7, v189, %13, %7\n\t" \
  "v_dot2_f32_bf16 %7, v190, %14, %7\n\t" \
  "v_dot2_f32_bf16 %7, v191, %15, %7\n\t" \
  "s_nop 2\n\t" \
  "v_add_f32_dpp %0, %0, %0 row_half_mirror row_mask:0xf bank_mask:0x5\n\t" \
  "v_add_f32_dpp %1, %1, %1 row_half_mirror row_mask:0xf bank_mask:0x5\n\t" \
  "v_add_f32_dpp %2, %2, %2 row_half_mirror row_mask:0xf bank_mask:0x5\n\t" \
  "v_add_f32_dpp %3, %3, %3 row_half_mirror row_mask:0xf bank_mask:0x5\n\t" \
  "v_add_f32_dpp %0, %4, %4 row_half_mirror row_mask:0xf bank_mask:0xa\n\t" \
  "v_add_f32_dpp %1, %5, %5 row_half_mirror row_mask:0xf bank_mask:0xa\n\t" \
  "v_add_f32_dpp %2, %6, %6 row_half_mirror row_mask:0xf bank_mask:0xa\n\t" \
  "v_add_f32_dpp %3, %7, %7 row_half_mirror row_mask:0xf bank_mask:0xa\n\t" \
  "s_nop 1\n\t" \
  : "=&v"(a0), "=&v"(a1), "=&v"(a2), "=&v"(a3), "=&v"(a4), "=&v"(a5), "=&v"(a6), "=&v"(a7) : "v"(q0), "v"(q1), "v"(q2), "v"(q3), "v"(q4), "v"(q5), "v"(q6), "v"(q7) : "memory")
#define PVALL(o0, o1, X, L) asm volatile( \
  "v_mov_b32_dpp v124, %2 row_ror:2 row_mask:0xf bank_mask:0x1\n\t" \
  "v_mov_b32_dpp v125, %2 row_ror:6 row_mask:0xf bank_mask:0x2\n\t" \
  "v_mov_b32_dpp v126, %2 row_ror:10 row_mask:0xf bank_mask:0x4\n\t" \
  "v_mov_b32_dpp v127, %2 row_ror:14 row_mask:0xf bank_mask:0x8\n\t" \
  "v_mov_b32_dpp v124, %2 row_ror:2 row_mask:0xf bank_mask:0x1\n\t" \
  "v_mov_b32_dpp v125, %2 row_ror:6 row_mask:0xf bank_mask:0x2\n\t" \
  "v_mfma_f32_16x16x32_bf16 v[56:59], v[120:123], v[240:243], v[56:59]\n\t" \
  "v_mov_b32_dpp v126, %2 row_ror:10 row_mask:0xf bank_mask:0x4\n\t" \
  "v_mov_b32_dpp v127, %2 row_ror:14 row_mask:0xf bank_mask:0x8\n\t" \
  "v_mfma_f32_16x16x32_bf16 v[60:63], v[120:123], v[244:247], v[60:63]\n\t" \
  "s_cbranch_scc1 2f\n\t" \
  "s_waitcnt vmcnt(0)\n\t" \
  "v_perm_b32 v64, v252, v248, %4\n\t" \
  "v_perm_b32 v252, v252, v248, %5\n\t" \
  "v_perm_b32 v65, v253, v249, %4\n\t" \
  "v_perm_b32 v253, v253, v249, %5\n\t" \
  "v_perm_b32 v66, v254, v250, %4\n\t" \
  "v_perm_b32 v254, v254, v250, %5\n\t" \
  "v_perm_b32 v67, v255, v251, %4\n\t" \
  "v_perm_b32 v255, v255, v251, %5\n\t" \
  "v_mov_b32 v248, v64\n\t" \
  "v_mov_b32 v249, v65\n\t" \
  "v_mov_b32 v250, v66\n\t" \
  "v_mov_b32 v251, v67\n\t" \
  "2:\n\t" \
  "s_nop 1\n\t" \
  "v_mfma_f32_16x16x32_bf16 v[56:59], v[124:127], v[248:251], v[56:59]\n\t" \
  "v_mfma_f32_16x16x32_bf16 v[60:63], v[124:127], v[252:255], v[60:63]\n\t" \
  "s_nop 7\n\t" \
  "v_add_f32 %0, v56, v57\n\t" \
  "v_add_f32 %1, v60, v61\n\t" \
  : "=&v"(o0), "=&v"(o1) : "v"(X), "s"(L), "s"(0x05040100u), "s"(0x07060302u) : "memory", "scc", "v56", "v57", "v58", "v59", "v60", "v61", "v62", "v63")
#define AZERO() asm volatile( \
  "v_mov_b32 v120, 0\n\t" \
  "v_mov_b32 v121, 0\n\t" \
  "v_mov_b32 v122, 0\n\t" \
  "v_mov_b32 v123, 0\n\t" \
  "v_mov_b32 v124, 0\n\t" \
  "v_mov_b32 v125, 0\n\t" \
  "v_mov_b32 v126, 0\n\t" \
  "v_mov_b32 v127, 0\n\t" \
  ::: "memory")
#define RWLOAD_W(vo, base) asm volatile( \
  "global_load_dwordx4 v[88:91], %0, %1 offset:0\n\t" \
  "global_load_dwordx4 v[92:95], %0, %1 offset:1280\n\t" \
  "global_load_dwordx4 v[96:99], %0, %1 offset:2560\n\t" \
  "global_load_dwordx4 v[100:103], %0, %1 offset:3840\n\t" \
  : "+v"(vo) : "s"(base) : "memory", "v255")
#define RWLOAD_M1A(vo, base) asm volatile( \
  "global_load_dwordx4 v[88:91], %0, %1 offset:0\n\t" \
  "global_load_dwordx4 v[92:95], %0, %1 offset:3456\n\t" \
  "v_add_u32 %0, 0x1b00, %0\n\t" \
  "global_load_dwordx4 v[96:99], %0, %1 offset:0\n\t" \
  "global_load_dwordx4 v[100:103], %0, %1 offset:3456\n\t" \
  "v_add_u32 %0, 0x1b00, %0\n\t" \
  "global_load_dwordx4 v[104:107], %0, %1 offset:0\n\t" \
  "global_load_dwordx4 v[108:111], %0, %1 offset:3456\n\t" \
  "v_add_u32 %0, 0x1b00, %0\n\t" \
  "global_load_dwordx4 v[112:115], %0, %1 offset:0\n\t" \
  "global_load_dwordx4 v[116:119], %0, %1 offset:3456\n\t" \
  : "+v"(vo) : "s"(base) : "memory", "v255")
#define RWLOAD_M1B(vo, base) asm volatile( \
  "global_load_dwordx4 v[80:83], %0, %1\n\t" \
  "global_load_dwordx4 v[84:87], %0, %1 offset:3456\n\t" \
  :: "v"(vo), "s"(base) : "memory", "v255")
#define RWLOAD_M2(vo, base) asm volatile( \
  "global_load_dwordx4 v[88:91], %0, %1 offset:0\n\t" \
  "global_load_dwordx4 v[92:95], %0, %1 offset:1280\n\t" \
  "global_load_dwordx4 v[96:99], %0, %1 offset:2560\n\t" \
  "global_load_dwordx4 v[100:103], %0, %1 offset:3840\n\t" \
  "v_add_u32 %0, 0x1400, %0\n\t" \
  "global_load_dwordx4 v[104:107], %0, %1 offset:0\n\t" \
  "global_load_dwordx4 v[108:111], %0, %1 offset:1280\n\t" \
  "global_load_dwordx4 v[112:115], %0, %1 offset:2560\n\t" \
  "global_load_dwordx4 v[116:119], %0, %1 offset:3840\n\t" \
  : "+v"(vo) : "s"(base) : "memory", "v255")
#define RWPERM4() asm volatile( \
  "s_waitcnt vmcnt(0)\n\t" \
  "v_perm_b32 v64, v92, v88, %0\n\t" \
  "v_perm_b32 v92, v92, v88, %1\n\t" \
  "v_perm_b32 v65, v93, v89, %0\n\t" \
  "v_perm_b32 v93, v93, v89, %1\n\t" \
  "v_perm_b32 v66, v94, v90, %0\n\t" \
  "v_perm_b32 v94, v94, v90, %1\n\t" \
  "v_perm_b32 v67, v95, v91, %0\n\t" \
  "v_perm_b32 v95, v95, v91, %1\n\t" \
  "v_mov_b32 v88, v64\n\t" \
  "v_mov_b32 v89, v65\n\t" \
  "v_mov_b32 v90, v66\n\t" \
  "v_mov_b32 v91, v67\n\t" \
  "v_perm_b32 v64, v100, v96, %0\n\t" \
  "v_perm_b32 v100, v100, v96, %1\n\t" \
  "v_perm_b32 v65, v101, v97, %0\n\t" \
  "v_perm_b32 v101, v101, v97, %1\n\t" \
  "v_perm_b32 v66, v102, v98, %0\n\t" \
  "v_perm_b32 v102, v102, v98, %1\n\t" \
  "v_perm_b32 v67, v103, v99, %0\n\t" \
  "v_perm_b32 v103, v103, v99, %1\n\t" \
  "v_mov_b32 v96, v64\n\t" \
  "v_mov_b32 v97, v65\n\t" \
  "v_mov_b32 v98, v66\n\t" \
  "v_mov_b32 v99, v67\n\t" \
  "v_perm_b32 v64, v108, v104, %0\n\t" \
  "v_perm_b32 v108, v108, v104, %1\n\t" \
  "v_perm_b32 v65, v109, v105, %0\n\t" \
  "v_perm_b32 v109, v109, v105, %1\n\t" \
  "v_perm_b32 v66, v110, v106, %0\n\t" \
  "v_perm_b32 v110, v110, v106, %1\n\t" \
  "v_perm_b32 v67, v111, v107, %0\n\t" \
  "v_perm_b32 v111, v111, v107, %1\n\t" \
  "v_mov_b32 v104, v64\n\t" \
  "v_mov_b32 v105, v65\n\t" \
  "v_mov_b32 v106, v66\n\t" \
  "v_mov_b32 v107, v67\n\t" \
  "v_perm_b32 v64, v116, v112, %0\n\t" \
  "v_perm_b32 v116, v116, v112, %1\n\t" \
  "v_perm_b32 v65, v117, v113, %0\n\t" \
  "v_perm_b32 v117, v117, v113, %1\n\t" \
  "v_perm_b32 v66, v118, v114, %0\n\t" \
  "v_perm_b32 v118, v118, v114, %1\n\t" \
  "v_perm_b32 v67, v119, v115, %0\n\t" \
  "v_perm_b32 v119, v119, v115, %1\n\t" \
  "v_mov_b32 v112, v64\n\t" \
  "v_mov_b32 v113, v65\n\t" \
  "v_mov_b32 v114, v66\n\t" \
  "v_mov_b32 v115, v67\n\t" \
  :: "s"(0x05040100u), "s"(0x07060302u) : "memory")
#define RWPERM_T() asm volatile( \
  "s_waitcnt vmcnt(0)\n\t" \
  "v_perm_b32 v64, v84, v80, %0\n\t" \
  "v_perm_b32 v84, v84, v80, %1\n\t" \
  "v_perm_b32 v65, v85, v81, %0\n\t" \
  "v_perm_b32 v85, v85, v81, %1\n\t" \
  "v_perm_b32 v66, v86, v82, %0\n\t" \
  "v_perm_b32 v86, v86, v82, %1\n\t" \
  "v_perm_b32 v67, v87, v83, %0\n\t" \
  "v_perm_b32 v87, v87, v83, %1\n\t" \
  "v_mov_b32 v80, v64\n\t" \
  "v_mov_b32 v81, v65\n\t" \
  "v_mov_b32 v82, v66\n\t" \
  "v_mov_b32 v83, v67\n\t" \
  :: "s"(0x05040100u), "s"(0x07060302u) : "memory")
#define RGEMV2(y0, y1) asm volatile( \
  "v_dot2_f32_bf16 %0, v88, %8, 0\n\t" \
  "v_dot2_f32_bf16 %1, v92, %8, 0\n\t" \
  "v_dot2_f32_bf16 %2, v89, %8, 0\n\t" \
  "v_dot2_f32_bf16 %3, v93, %8, 0\n\t" \
  "v_dot2_f32_bf16 %4, v90, %8, 0\n\t" \
  "v_dot2_f32_bf16 %5, v94, %8, 0\n\t" \
  "v_dot2_f32_bf16 %6, v91, %8, 0\n\t" \
  "v_dot2_f32_bf16 %7, v95, %8, 0\n\t" \
  "v_dot2_f32_bf16 %0, v96, %9, %0\n\t" \
  "v_dot2_f32_bf16 %1, v100, %9, %1\n\t" \
  "v_dot2_f32_bf16 %2, v97, %9, %2\n\t" \
  "v_dot2_f32_bf16 %3, v101, %9, %3\n\t" \
  "v_dot2_f32_bf16 %4, v98, %9, %4\n\t" \
  "v_dot2_f32_bf16 %5, v102, %9, %5\n\t" \
  "v_dot2_f32_bf16 %6, v99, %9, %6\n\t" \
  "v_dot2_f32_bf16 %7, v103, %9, %7\n\t" \
  "s_nop 2\n\t" \
  : "=&v"(a0), "=&v"(a1), "=&v"(a2), "=&v"(a3), "=&v"(a4), "=&v"(a5), "=&v"(a6), "=&v"(a7) : "v"(y0), "v"(y1) : "memory")
#define RGEMV4(y0, y1, y2, y3) asm volatile( \
  "v_dot2_f32_bf16 %0, v88, %8, 0\n\t" \
  "v_dot2_f32_bf16 %1, v92, %8, 0\n\t" \
  "v_dot2_f32_bf16 %2, v89, %8, 0\n\t" \
  "v_dot2_f32_bf16 %3, v93, %8, 0\n\t" \
  "v_dot2_f32_bf16 %4, v90, %8, 0\n\t" \
  "v_dot2_f32_bf16 %5, v94, %8, 0\n\t" \
  "v_dot2_f32_bf16 %6, v91, %8, 0\n\t" \
  "v_dot2_f32_bf16 %7, v95, %8, 0\n\t" \
  "v_dot2_f32_bf16 %0, v96, %9, %0\n\t" \
  "v_dot2_f32_bf16 %1, v100, %9, %1\n\t" \
  "v_dot2_f32_bf16 %2, v97, %9, %2\n\t" \
  "v_dot2_f32_bf16 %3, v101, %9, %3\n\t" \
  "v_dot2_f32_bf16 %4, v98, %9, %4\n\t" \
  "v_dot2_f32_bf16 %5, v102, %9, %5\n\t" \
  "v_dot2_f32_bf16 %6, v99, %9, %6\n\t" \
  "v_dot2_f32_bf16 %7, v103, %9, %7\n\t" \
  "v_dot2_f32_bf16 %0, v104, %10, %0\n\t" \
  "v_dot2_f32_bf16 %1, v108, %10, %1\n\t" \
  "v_dot2_f32_bf16 %2, v105, %10, %2\n\t" \
  "v_dot2_f32_bf16 %3, v109, %10, %3\n\t" \
  "v_dot2_f32_bf16 %4, v106, %10, %4\n\t" \
  "v_dot2_f32_bf16 %5, v110, %10, %5\n\t" \
  "v_dot2_f32_bf16 %6, v107, %10, %6\n\t" \
  "v_dot2_f32_bf16 %7, v111, %10, %7\n\t" \
  "v_dot2_f32_bf16 %0, v112, %11, %0\n\t" \
  "v_dot2_f32_bf16 %1, v116, %11, %1\n\t" \
  "v_dot2_f32_bf16 %2, v113, %11, %2\n\t" \
  "v_dot2_f32_bf16 %3, v117, %11, %3\n\t" \
  "v_dot2_f32_bf16 %4, v114, %11, %4\n\t" \
  "v_dot2_f32_bf16 %5, v118, %11, %5\n\t" \
  "v_dot2_f32_bf16 %6, v115, %11, %6\n\t" \
  "v_dot2_f32_bf16 %7, v119, %11, %7\n\t" \
  "s_nop 2\n\t" \
  : "=&v"(a0), "=&v"(a1), "=&v"(a2), "=&v"(a3), "=&v"(a4), "=&v"(a5), "=&v"(a6), "=&v"(a7) : "v"(y0), "v"(y1), "v"(y2), "v"(y3) : "memory")
#define RGEMV5(y0, y1, y2, y3, y4) asm volatile( \
  "v_dot2_f32_bf16 %0, v88, %8, 0\n\t" \
  "v_dot2_f32_bf16 %1, v92, %8, 0\n\t" \
  "v_dot2_f32_bf16 %2, v89, %8, 0\n\t" \
  "v_dot2_f32_bf16 %3, v93, %8, 0\n\t" \
  "v_dot2_f32_bf16 %4, v90, %8, 0\n\t" \
  "v_dot2_f32_bf16 %5, v94, %8, 0\n\t" \
  "v_dot2_f32_bf16 %6, v91, %8, 0\n\t" \
  "v_dot2_f32_bf16 %7, v95, %8, 0\n\t" \
  "v_dot2_f32_bf16 %0, v96, %9, %0\n\t" \
  "v_dot2_f32_bf16 %1, v100, %9, %1\n\t" \
  "v_dot2_f32_bf16 %2, v97, %9, %2\n\t" \
  "v_dot2_f32_bf16 %3, v101, %9, %3\n\t" \
  "v_dot2_f32_bf16 %4, v98, %9, %4\n\t" \
  "v_dot2_f32_bf16 %5, v102, %9, %5\n\t" \
  "v_dot2_f32_bf16 %6, v99, %9, %6\n\t" \
  "v_dot2_f32_bf16 %7, v103, %9, %7\n\t" \
  "v_dot2_f32_bf16 %0, v104, %10, %0\n\t" \
  "v_dot2_f32_bf16 %1, v108, %10, %1\n\t" \
  "v_dot2_f32_bf16 %2, v105, %10, %2\n\t" \
  "v_dot2_f32_bf16 %3, v109, %10, %3\n\t" \
  "v_dot2_f32_bf16 %4, v106, %10, %4\n\t" \
  "v_dot2_f32_bf16 %5, v110, %10, %5\n\t" \
  "v_dot2_f32_bf16 %6, v107, %10, %6\n\t" \
  "v_dot2_f32_bf16 %7, v111, %10, %7\n\t" \
  "v_dot2_f32_bf16 %0, v112, %11, %0\n\t" \
  "v_dot2_f32_bf16 %1, v116, %11, %1\n\t" \
  "v_dot2_f32_bf16 %2, v113, %11, %2\n\t" \
  "v_dot2_f32_bf16 %3, v117, %11, %3\n\t" \
  "v_dot2_f32_bf16 %4, v114, %11, %4\n\t" \
  "v_dot2_f32_bf16 %5, v118, %11, %5\n\t" \
  "v_dot2_f32_bf16 %6, v115, %11, %6\n\t" \
  "v_dot2_f32_bf16 %7, v119, %11, %7\n\t" \
  "v_dot2_f32_bf16 %0, v80, %12, %0\n\t" \
  "v_dot2_f32_bf16 %1, v84, %12, %1\n\t" \
  "v_dot2_f32_bf16 %2, v81, %12, %2\n\t" \
  "v_dot2_f32_bf16 %3, v85, %12, %3\n\t" \
  "v_dot2_f32_bf16 %4, v82, %12, %4\n\t" \
  "v_dot2_f32_bf16 %5, v86, %12, %5\n\t" \
  "v_dot2_f32_bf16 %6, v83, %12, %6\n\t" \
  "v_dot2_f32_bf16 %7, v87, %12, %7\n\t" \
  "s_nop 2\n\t" \
  : "=&v"(a0), "=&v"(a1), "=&v"(a2), "=&v"(a3), "=&v"(a4), "=&v"(a5), "=&v"(a6), "=&v"(a7) : "v"(y0), "v"(y1), "v"(y2), "v"(y3), "v"(y4) : "memory")

#define STAMP(i) do {} while (0)

__global__ void __launch_bounds__(512, 2) __attribute__((amdgpu_num_vgpr(32)))
fused_kernel(const char* __restrict__ xin, const char* __restrict__ Kc, const char* __restrict__ Vc,
             const char* __restrict__ Wq, const char* __restrict__ Wo, const char* __restrict__ W1,
             const char* __restrict__ B1, const char* __restrict__ W2, const char* __restrict__ B2,
             char* __restrict__ out, char* __restrict__ ws) {
  __shared__ Lds S;
  const int tid = threadIdx.x, lane0 = tid & 63;
  const int w = __builtin_amdgcn_readfirstlane(tid >> 6);
  const int b = blockIdx.x, g = b & 7, j = b >> 3, h = 4 * g + (j >> 3), s = j & 7;
  const int m1 = (j - 20) * 8 + g, m2 = g * 5 + (j - 27);
  const int role = j < 20 ? 0 : (j < 27 ? (m1 < 54 ? 1 : 3) : 2);
  const rsrc_t R = __builtin_amdgcn_make_buffer_rsrc(ws, 0, 0x200000, 0x00020000);
  const size_t kvoff = (size_t)(h * 8 + s) * 131072 + (size_t)w * 16384;
  u32 bias = 0;
  u32 x0 = 0, x1 = 0, x2 = 0, x3 = 0, x4 = 0, E = 0;

  {
    const int lane = lane0;
    if (w < 4) {
      const int sg = 16 * w + (lane >> 2);
      u32 o0 = 8 * sg * 8192 + (lane & 3) * 8, o1 = (512 + 2 * sg) * 8192 + (lane & 3) * 8;
      const char* wb = Wq + h * 256 + s * 32;
      WQLOAD(o0, o1, wb);
    } else if (w == 4) {
      __builtin_amdgcn_s_setprio(3);
      u32x4 xa = *(const u32x4*)(xin + lane * 16);
      x4 = *(const u32*)(xin + 1024 + lane * 4);
      E = ld32s(R, 0, WS_E);
      x0 = xa.x; x1 = xa.y; x2 = xa.z; x3 = xa.w;
    } else if (w == 7) {
      __builtin_amdgcn_s_setprio(3);
      const u32x4 ff = {SENT, SENT, SENT, SENT};
      u32 vo, so; bool act;
      if (role == 0) { vo = lane * 4; so = O_Z + g * 2560 + j * 128; act = lane < 32; }
      else if (role == 1) { vo = lane * 4; so = O_H + m1 * 128; act = lane < 16; }
      else { vo = (lane >> 3) * 8192 + (lane & 7) * 4; so = O_X + m2 * 128; act = role == 2; }
      if (lane < 33) {
        st128s(ff, R, lane * 16, WS_LB + O_REC + (h * 8 + s) * 640);
        st128s(ff, R, lane * 16, WS_LB + WS_LSTRIDE + O_REC + (h * 8 + s) * 640);
      }
      if (act) {
        st32s(SENT, R, vo, WS_LB + so);
        st32s(SENT, R, vo, WS_LB + WS_LSTRIDE + so);
      }
      __builtin_amdgcn_s_setprio(0);
    } else if (w == 5) {
      if (lane < 16) ((u32*)S.mw)[lane] = SENT;
      if (lane < 7) (&S.f_y)[lane] = 0;
    }
    AZERO();
    asm volatile("s_waitcnt lgkmcnt(0)\n\ts_barrier" ::: "memory");
    if (w < 4) {
      const u32 kvo = (32 * ((lane >> 3) & 1) + (lane >> 4)) * 256 + (lane & 7) * 16;
      const char* kb = Kc + kvoff + 4096;
      KLOAD(kvo, kb);
      __builtin_amdgcn_s_setprio(2);
    }
    if (w == 7) {
      asm volatile("s_waitcnt vmcnt(0)" ::: "memory");
      lds_wait(&S.f_y, 1);
      if (lane == 0) st32s(S.tag, R, 0, WS_FLAGS + b * 4);
    }
  }

#pragma unroll 1
  for (int L = 0; L < 2; L++) {
    const u32 lb = WS_LB + L * WS_LSTRIDE;
    if (w == 4) {
      int lane = lane0; OPAQUE(lane);
      if (L == 1) {
        const u32 xo = WS_LB + O_X + g * 8192;
        int sp = 0;
        while (true) {
          CBAR();
          u32x4 xa = ld128s(R, (lane >> 1) * 128 + (lane & 1) * 16, xo);
          x4 = ld32s(R, (32 + (lane >> 3)) * 128 + (lane & 7) * 4, xo);
          x0 = xa.x; x1 = xa.y; x2 = xa.z; x3 = xa.w;
          u32 mx = umax3(umax3(x0, x1, x2), x3, x4);
          if (__builtin_amdgcn_ballot_w64(mx == SENT) == 0 || ++sp > SPIN_G) break;
        }
      }
      STAMP(1);
      *(u32x4*)&S.xs[4 * lane] = u32x4{x0, x1, x2, x3};
      S.xs[256 + lane] = x4;
      LN_PASS(x0, x1, x2, x3, x4);
      *(u32x4*)&S.ys[4 * lane] = u32x4{x0, x1, x2, x3};
      S.ys[256 + lane] = x4;
      const u32 tag = E + 0x7F4A7C15u;
      S.tag = tag;
      CBAR();
      *(u32*)&S.f_y = L + 1;
      CBAR();
      STAMP(2);
      u32x2 q;
      int sp = 0;
      while (true) {
        CBAR();
        q = ld64s(R, (lane >> 3) * 128 + (lane & 7) * 8, lb + O_Q + h * 1024);
        if (__builtin_amdgcn_ballot_w64(q.y != tag) == 0 || ++sp > SPIN_G) break;
      }
      S.qs[lane] = q.x;
      CBAR();
      *(u32*)&S.f_q = L + 1;
      CBAR();
      __builtin_amdgcn_s_setprio(0);
      STAMP(4);
    } else if (w < 4) {
      int lane = lane0; OPAQUE(lane);
      if (L == 0) WQPERM();
      const int sg = 16 * w + (lane >> 2);
      lds_wait(&S.f_y, L + 1);
      u32x4 y4 = *(const u32x4*)&S.ys[4 * sg];
      u32 y0 = y4.x, y1 = y4.y, y2 = y4.z, y3 = y4.w, y5 = S.ys[256 + sg];
      float a0, a1, a2, a3;
      QGEMV(a0, a1, a2, a3, y0, y1, y2, y3, y5);
      float t = red16(red32(a0, a2), red32(a1, a3));
      t += dppf<ROR4>(t);
      t += dppf<ROR8>(t);
      if ((lane & 12) == 0) S.qpart[w][4 * (lane & 3) + (lane >> 4)] = t;
      u32 c = lds_inc(&S.c_q);
      if (c == 4 * L + 3) {
        if (lane < 8) {
          f32x2 q0 = *(const f32x2*)&S.qpart[0][2 * lane], q1 = *(const f32x2*)&S.qpart[1][2 * lane];
          f32x2 q2 = *(const f32x2*)&S.qpart[2][2 * lane], q3 = *(const f32x2*)&S.qpart[3][2 * lane];
          f32x2 q = (q0 + q1) + (q2 + q3);
          st64s(u32x2{pk(q.x, q.y), S.tag}, R, lane * 8, lb + O_Q + (h * 8 + s) * 128);
        }
        STAMP(3);
      }
      __builtin_amdgcn_s_setprio(0);
    }
    int lane = lane0; OPAQUE(lane);
    if (L == 0) {
      if (w >= 4) {
        const u32 kvo = (32 * ((lane >> 3) & 1) + (lane >> 4)) * 256 + (lane & 7) * 16;
        const char* kb = Kc + kvoff + 4096;
        KLOAD(kvo, kb);
      }
      const u32 vvo = lane * 16;
      const char* vb0 = Vc + kvoff + 4096;
      const char* vb1 = Vc + kvoff + 12288;
      VLOAD(vvo, vb0, vb1);
    }
    lds_wait(&S.f_q, L + 1);
    STAMP(5);
    float sc;
    {
      const int ch = lane & 7;
      u32x4 qa = *(const u32x4*)&S.qs[4 * ch], qb = *(const u32x4*)&S.qs[32 + 4 * ch];
      u32 q0 = qa.x, q1 = qa.y, q2 = qa.z, q3 = qa.w, q4 = qb.x, q5 = qb.y, q6 = qb.z, q7 = qb.w;
      float a0, a1, a2, a3, a4, a5, a6, a7;
      QK();
      const bool b1 = lane & 2, b0 = lane & 1;
      float t0 = (b1 ? a2 : a0) + dppf<QP_X2>(b1 ? a0 : a2);
      float t1 = (b1 ? a3 : a1) + dppf<QP_X2>(b1 ? a1 : a3);
      sc = (b0 ? t1 : t0) + dppf<QP_X1>(b0 ? t0 : t1);
      sc = rb(rb(sc) * 0.08838834764831845f);
    }
    float mx = fmx(sc, dppf<QP_X1>(sc));
    mx = fmx(mx, dppf<QP_X2>(mx));
    mx = fmx(mx, dppf<ROR4>(mx));
    mx = fmx(mx, dppf<ROR8>(mx));
    const float mwv = fmx(fmx(rdlane(mx, 0), rdlane(mx, 16)), fmx(rdlane(mx, 32), rdlane(mx, 48)));
    *(u32*)&S.mw[L][w] = clampS(f2u(mwv)); CBAR();
    const float p = __builtin_amdgcn_exp2f((sc - mwv) * LOG2E);
    u32 X;
    {
      const bool odd = lane & 1;
      float lo = p - hi2f(f2u(p));
      float own = odd ? lo : p, snd = odd ? p : lo;
      u32 rcv = dppu<QP_X1>(f2u(snd));
      X = __builtin_amdgcn_perm(rcv, f2u(own), odd ? 0x03020706u : 0x07060302u);
    }
    float ls = p + dppf<QP_X1>(p);
    ls += dppf<QP_X2>(ls);
    ls += dppf<ROR4>(ls);
    ls += dppf<ROR8>(ls);
    const float lw = (rdlane(ls, 0) + rdlane(ls, 16)) + (rdlane(ls, 32) + rdlane(ls, 48));
    STAMP(6);
    float oe, oo;
    PVALL(oe, oo, X, L);
    STAMP(7);
    {
      float M;
      int sp = 0;
      while (true) {
        CBAR();
        u32x4 m0 = *(u32x4*)&S.mw[L][0], m1v = *(u32x4*)&S.mw[L][4];
        u32 um = umax3(umax3(m0.x, m0.y, m0.z), umax3(m0.w, m1v.x, m1v.y), umax3(m1v.z, m1v.w, 0u));
        M = fmx(fmx(fmx(u2f(m0.x), u2f(m0.y)), fmx(u2f(m0.z), u2f(m0.w))), fmx(fmx(u2f(m1v.x), u2f(m1v.y)), fmx(u2f(m1v.z), u2f(m1v.w))));
        if (__builtin_amdgcn_readfirstlane(um) != SENT || ++sp > SPIN_L) break;
      }
      const float f = __builtin_amdgcn_exp2f((mwv - M) * LOG2E);
      f32x2 o = {oe * f, oo * f};
      *(f32x2*)&S.part[w][8 * (lane & 15) + 2 * (lane >> 4)] = o;
      if (lane == 0) *(f32x4*)&S.part[w][128] = f32x4{lw * f, w == 0 ? M : 0.f, 0.f, 0.f};
      u32 c = lds_inc(&S.c_part);
      if (c == 8 * L + 7) {
        if (lane < 33) {
          const f32x4* pr = (const f32x4*)&S.part[0][4 * lane];
          f32x4 t = ((pr[0] + pr[33]) + (pr[66] + pr[99])) + ((pr[132] + pr[165]) + (pr[198] + pr[231]));
          st128s(u32x4{clampS(f2u(t.x)), clampS(f2u(t.y)), clampS(f2u(t.z)), clampS(f2u(t.w))}, R, lane * 16, lb + O_REC + (h * 8 + s) * 640);
        }
        STAMP(8);
      }
    }
    if (L == 0) {
      if (role == 0) {
        u32 vo = (64 * w + 16 * (lane >> 4) + 4 * (lane & 3)) * 1280 + ((lane >> 2) & 3) * 16;
        const char* base = Wo + (size_t)(512 * g) * 1280 + j * 64;
        RWLOAD_W(vo, base);
      } else if (role == 1) {
        if (w >= 4) {
          const int sg = 16 * (w - 4) + (lane >> 2);
          u32 vo = 8 * sg * 3456 + (lane & 3) * 16, vo2 = (512 + 2 * sg) * 3456 + (lane & 3) * 16;
          const char* base = W1 + m1 * 64;
          RWLOAD_M1A(vo, base);
          RWLOAD_M1B(vo2, base);
          bias = *(const u32*)(B1 + m1 * 64 + (lane & 15) * 4);
          RWPERM_T();
        }
      } else if (role == 2) {
        const int rg = (lane >> 1) < 27 ? (lane >> 1) : 26;
        u32 vo = (216 * w + 8 * rg) * 1280 + (lane & 1) * 16;
        const char* base = W2 + m2 * 32;
        RWLOAD_M2(vo, base);
        bias = *(const u32*)(B2 + m2 * 32 + (lane & 7) * 4);
      }
      if (w == 1) {
        const u32 tag = S.tag;
        int sp = 0;
        while (true) {
          CBAR();
          u32x4 f = ld128s(R, lane * 16, WS_FLAGS);
          bool ok = f.x == tag && f.y == tag && f.z == tag && f.w == tag;
          if (__builtin_amdgcn_ballot_w64(!ok) == 0 || ++sp > SPIN_G) break;
        }
        CBAR(); S.f_ep = 1; CBAR();
        if (b == 0 && lane == 0) st32s(tag, R, 0, WS_E);
      }
      RWPERM4();
      lds_wait(&S.f_ep, 1);
    }
    STAMP(9);
    float a0, a1, a2, a3, a4, a5, a6, a7;
    if (role == 0) {
      const int c = lane >> 4, sq = (lane >> 2) & 3, e = lane & 3;
      const u32 rb0 = lb + O_REC + (4 * g + (w >> 1)) * 8 * 640;
      const u32 va = sq * 640 + (w & 1) * 256 + (4 * c + e) * 16, vm = (sq + 4 * (e & 1)) * 640 + 512;
      u32x4 A, Bv; u32x2 ML;
      int sp = 0;
      while (true) {
        CBAR();
        A = ld128s(R, va, rb0);
        Bv = ld128s(R, va + 2560, rb0);
        ML = ld64s(R, vm, rb0);
        u32 mxu = umax3(umax3(A.x, A.y, A.z), umax3(A.w, Bv.x, Bv.y), umax3(umax3(Bv.z, Bv.w, ML.x), ML.y, 0u));
        if (__builtin_amdgcn_ballot_w64(mxu == SENT) == 0 || ++sp > SPIN_G) break;
      }
      STAMP(10);
      const float Mr = u2f(ML.y);
      float Mx = fmx(Mr, dppf<QP_X1>(Mr));
      Mx = fmx(Mx, dppf<ROR4>(Mx));
      Mx = fmx(Mx, dppf<ROR8>(Mx));
      const float f = __builtin_amdgcn_exp2f((Mr - Mx) * LOG2E);
      float Ls = u2f(ML.x) * f;
      Ls += dppf<QP_X1>(Ls);
      Ls += dppf<ROR4>(Ls);
      Ls += dppf<ROR8>(Ls);
      const float ri = __builtin_amdgcn_rcpf(Ls);
      const float fa = dppf<QP_B0>(f), fb = dppf<QP_B1>(f);
      float o0 = u2f(A.x) * fa + u2f(Bv.x) * fb, o1 = u2f(A.y) * fa + u2f(Bv.y) * fb;
      float o2 = u2f(A.z) * fa + u2f(Bv.z) * fb, o3 = u2f(A.w) * fa + u2f(Bv.w) * fb;
      o0 += dppf<ROR4>(o0); o1 += dppf<ROR4>(o1); o2 += dppf<ROR4>(o2); o3 += dppf<ROR4>(o3);
      o0 += dppf<ROR8>(o0); o1 += dppf<ROR8>(o1); o2 += dppf<ROR8>(o2); o3 += dppf<ROR8>(o3);
      const u32 p0 = pk(o0 * ri, o1 * ri), p1 = pk(o2 * ri, o3 * ri);
      RGEMV2(p0, p1);
      float t0 = red16(red32(a0, a4), red32(a2, a6)), t1 = red16(red32(a1, a5), red32(a3, a7));
      t0 += dppf<QP_X1>(t0); t1 += dppf<QP_X1>(t1);
      t0 += dppf<QP_X2>(t0); t1 += dppf<QP_X2>(t1);
      if (e == 0) *(f32x2*)&S.wpart[w][8 * sq + 2 * c] = f32x2{t0, t1};
      u32 cc = lds_inc(&S.c_role);
      if (cc == 8 * L + 7) {
        if (lane < 32) {
          const float* r = &S.wpart[0][lane];
          float t = ((r[0] + r[32]) + (r[64] + r[96])) + ((r[128] + r[160]) + (r[192] + r[224]));
          st32s(clampS(f2u(t)), R, lane * 4, lb + O_Z + g * 2560 + j * 128);
        }
        STAMP(11);
      }
    } else if (role == 1) {
      if (w < 5) {
        const int hb = lane >> 5, i = lane & 31;
        const u32 xp = S.xs[64 * w + 2 * i + hb];
        const u32 va = hb * 2560 + w * 512 + i * 16;
        u32x4 z0, z1, z2, z3;
        int sp = 0;
        while (true) {
          CBAR();
          z0 = ld128s(R, va, lb + O_Z); z1 = ld128s(R, va, lb + O_Z + 5120);
          z2 = ld128s(R, va, lb + O_Z + 10240); z3 = ld128s(R, va, lb + O_Z + 15360);
          u32 mxu = umax3(umax3(umax3(z0.x, z0.y, z0.z), umax3(z0.w, z1.x, z1.y), umax3(z1.z, z1.w, z2.x)),
                          umax3(umax3(z2.y, z2.z, z2.w), umax3(z3.x, z3.y, z3.z), z3.w), 0u);
          if (__builtin_amdgcn_ballot_w64(mxu == SENT) == 0 || ++sp > SPIN_G) break;
        }
        STAMP(10);
        f32x4 zs = (__builtin_bit_cast(f32x4, z0) + __builtin_bit_cast(f32x4, z1)) + (__builtin_bit_cast(f32x4, z2) + __builtin_bit_cast(f32x4, z3));
        float za = red32(zs.x, zs.z), zb = red32(zs.y, zs.w);
        S.x1s[64 * w + 2 * i + hb] = pk(lo2f(xp) + rb(za), hi2f(xp) + rb(zb));
        lds_inc_nr(&S.c_x1);
      }
      if (w >= 4) {
        const int sg = 16 * (((lane & 3) + w) & 3) + (lane >> 2);
        lds_wait(&S.c_x1, 5 * (L + 1));
        u32x4 xv = *(const u32x4*)&S.x1s[4 * sg];
        u32 p0 = xv.x, p1 = xv.y, p2 = xv.z, p3 = xv.w, p4 = S.x1s[256 + sg];
#pragma unroll 1
        for (int it = 0; it < 2; it++) LN_PASS(p0, p1, p2, p3, p4);
        p0 = dppu<QP_B0>(p0); p1 = dppu<QP_B0>(p1); p2 = dppu<QP_B0>(p2); p3 = dppu<QP_B0>(p3); p4 = dppu<QP_B0>(p4);
        RGEMV5(p0, p1, p2, p3, p4);
        float t0 = red16(red32(a0, a4), red32(a2, a6)), t1 = red16(red32(a1, a5), red32(a3, a7));
        t0 += dppf<ROR4>(t0); t1 += dppf<ROR4>(t1);
        t0 += dppf<ROR8>(t0); t1 += dppf<ROR8>(t1);
        if ((lane & 12) == 0) *(f32x2*)&S.wpart[w - 4][8 * (lane & 3) + 2 * (lane >> 4)] = f32x2{t0, t1};
        u32 cc = lds_inc(&S.c_role);
        if (cc == 4 * L + 3) {
          if (lane < 16) {
            const f32x2* r = (const f32x2*)&S.wpart[0][2 * lane];
            f32x2 t = (r[0] + r[16]) + (r[32] + r[48]);
            float g0 = rb(rb(t.x) + lo2f(bias)), g1 = rb(rb(t.y) + hi2f(bias));
            const float C1 = -2.f * 0.7978845608028654f * LOG2E, C3 = C1 * 0.044715f;
            float e0 = __builtin_amdgcn_exp2f(g0 * __builtin_fmaf(C3 * g0, g0, C1));
            float e1 = __builtin_amdgcn_exp2f(g1 * __builtin_fmaf(C3 * g1, g1, C1));
            float h0 = g0 * __builtin_amdgcn_rcpf(1.f + e0), h1 = g1 * __builtin_amdgcn_rcpf(1.f + e1);
            st32s(clampS(pk(h0, h1)), R, lane * 4, lb + O_H + m1 * 128);
          }
          STAMP(11);
        }
      }
    } else if (role == 2) {
      if (w == 0) {
        const u32 xp = S.xs[8 * m2 + (lane & 7)];
        u32x2 z;
        int sp = 0;
        while (true) {
          CBAR();
          z = ld64s(R, (lane >> 3) * 2560 + (lane & 7) * 8, lb + O_Z + m2 * 64);
          if (__builtin_amdgcn_ballot_w64(umax3(z.x, z.y, 0u) == SENT) == 0 || ++sp > SPIN_G) break;
        }
        float za = u2f(z.x), zb = u2f(z.y);
        za = red16(za, za); zb = red16(zb, zb);
        za = red32(za, za); zb = red32(zb, zb);
        za += dppf<ROR8>(za); zb += dppf<ROR8>(zb);
        if (lane < 8) S.x1m[lane] = pk(lo2f(xp) + rb(za), hi2f(xp) + rb(zb));
      }
      const int rg = (lane >> 1) < 27 ? (lane >> 1) : 26;
      const int kk = 27 * w + rg;
      const u32 va = (kk >> 2) * 128 + (kk & 3) * 16;
      u32x4 hv;
      int sp = 0;
      while (true) {
        CBAR();
        hv = ld128s(R, va, lb + O_H);
        if (__builtin_amdgcn_ballot_w64(umax3(umax3(hv.x, hv.y, hv.z), hv.w, 0u) == SENT) == 0 || ++sp > SPIN_G) break;
      }
      STAMP(10);
      if (lane >= 54) hv = u32x4{0, 0, 0, 0};
      const u32 p0 = hv.x, p1 = hv.y, p2 = hv.z, p3 = hv.w;
      RGEMV4(p0, p1, p2, p3);
      float t0 = red16(red32(a0, a4), red32(a2, a6)), t1 = red16(red32(a1, a5), red32(a3, a7));
      t0 += dppf<QP_X2>(t0); t1 += dppf<QP_X2>(t1);
      t0 += dppf<ROR4>(t0); t1 += dppf<ROR4>(t1);
      t0 += dppf<ROR8>(t0); t1 += dppf<ROR8>(t1);
      if ((lane & 14) == 0) *(f32x2*)&S.wpart[w][8 * (lane & 1) + 2 * (lane >> 4)] = f32x2{t0, t1};
      u32 cc = lds_inc(&S.c_role);
      if (cc == 8 * L + 7) {
        const int d = lane & 7;
        const f32x2* r = (const f32x2*)&S.wpart[0][2 * d];
        const u32 xr = S.x1m[d];
        f32x2 t = ((r[0] + r[16]) + (r[32] + r[48])) + ((r[64] + r[80]) + (r[96] + r[112]));
        const u32 o = pk(lo2f(xr) + rb(rb(t.x) + lo2f(bias)), hi2f(xr) + rb(rb(t.y) + hi2f(bias)));
        if (L == 0) st32s(clampS(o), R, (lane >> 3) * 8192 + d * 4, lb + O_X + m2 * 128);
        else if (lane < 8) *(u32*)(out + m2 * 32 + d * 4) = o;
        STAMP(11);
      }
    }
  }
}

extern "C" void kernel_launch(void* const* d_in, const int* in_sizes, int n_in, void* d_out, int out_size,
                              void* d_ws, size_t ws_size, hipStream_t stream) {
  fused_kernel<<<256, 512, 0, stream>>>((const char*)d_in[0], (const char*)d_in[1], (const char*)d_in[2],
                                        (const char*)d_in[3], (const char*)d_in[4], (const char*)d_in[6],
                                        (const char*)d_in[7], (const char*)d_in[8], (const char*)d_in[9],
                                        (char*)d_out, (char*)d_ws);
}
```

```cpp
#include <hip/hip_runtime.h>
#include <stdint.h>

typedef unsigned u32;
typedef unsigned long long u64;
typedef u32 u32x4 __attribute__((ext_vector_type(4)));
typedef u32 u32x2 __attribute__((ext_vector_type(2)));
typedef float f32x4 __attribute__((ext_vector_type(4)));
typedef float f32x2 __attribute__((ext_vector_type(2)));
typedef __bf16 bf16x2 __attribute__((ext_vector_type(2)));
typedef __bf16 bf16x8 __attribute__((ext_vector_type(8)));
typedef __amdgpu_buffer_rsrc_t rsrc_t;

#define DEV __device__ __forceinline__
#define CBAR() asm volatile("" ::: "memory")
#define SB() __builtin_amdgcn_sched_barrier(0)

#define DIAG 0

constexpr u32 WS_E = 0;
constexpr u32 WS_FLAGS = 4096;
constexpr u32 WS_LB = 16384;
constexpr u32 WS_LSTRIDE = 0x50000;
constexpr u32 O_REC = 0;
constexpr u32 O_Z = 0x28000;
constexpr u32 O_H = 0x2E000;
constexpr u32 O_X = 0x30000;
constexpr u32 O_Q = 0x40000;
constexpr u32 WS_DIAG = 0x100000;
constexpr u32 SENT = 0xFFFFFFFFu;
constexpr int SPIN_G = 1 << 16;
constexpr int SPIN_L = 1 << 22;
constexpr float LOG2E = 1.4426950408889634f;

DEV float u2f(u32 v) { return __builtin_bit_cast(float, v); }
DEV u32 f2u(float v) { return __builtin_bit_cast(u32, v); }
DEV float dot2(u32 a, u32 b, float c) {
  return __builtin_amdgcn_fdot2_f32_bf16(__builtin_bit_cast(bf16x2, a), __builtin_bit_cast(bf16x2, b), c, false);
}
DEV u32 pk(float lo, float hi) {
  f32x2 t = {lo, hi};
  bf16x2 p = __builtin_convertvector(t, bf16x2);
  return __builtin_bit_cast(u32, p);
}
DEV float rb(float x) { return u2f(pk(0.f, x)); }
DEV float lo2f(u32 p) { return u2f(p << 16); }
DEV float hi2f(u32 p) { return u2f(p & 0xffff0000u); }
template <int CTRL> DEV float dppf(float x) { return u2f((u32)__builtin_amdgcn_mov_dpp((int)f2u(x), CTRL, 0xf, 0xf, true)); }
template <int CTRL> DEV u32 dppu(u32 x) { return (u32)__builtin_amdgcn_mov_dpp((int)x, CTRL, 0xf, 0xf, true); }
constexpr int QP_X1 = 0xB1, QP_X2 = 0x4E, QP_B0 = 0x00, QP_B1 = 0x55, ROR4 = 0x124, ROR8 = 0x128, HMIR = 0x141;
DEV float red32(float a, float b) {
  auto s = __builtin_amdgcn_permlane32_swap(f2u(a), f2u(b), false, false);
  u32 x = s[0], y = s[1];
  return u2f(x) + u2f(y);
}
DEV float red16(float a, float b) {
  auto s = __builtin_amdgcn_permlane16_swap(f2u(a), f2u(b), false, false);
  u32 x = s[0], y = s[1];
  return u2f(x) + u2f(y);
}
DEV float rdlane(float x, int l) { return u2f((u32)__builtin_amdgcn_readlane((int)f2u(x), l)); }
DEV u32 umax3(u32 a, u32 b, u32 c) { u32 m = a > b ? a : b; return m > c ? m : c; }
DEV u32 clampS(u32 x) { return x < 0xFFFFFFFEu ? x : 0xFFFFFFFEu; }
DEV u32 plo(u32 a, u32 b) { return __builtin_amdgcn_perm(b, a, 0x05040100u); }
DEV u32 phi(u32 a, u32 b) { return __builtin_amdgcn_perm(b, a, 0x07060302u); }
DEV u64 rt() { return __builtin_amdgcn_s_memrealtime(); }

DEV u32x4 ld128s(rsrc_t r, u32 v, u32 s) { return __builtin_amdgcn_raw_buffer_load_b128(r, v, s, 16); }
DEV u32x2 ld64s(rsrc_t r, u32 v, u32 s) { return __builtin_amdgcn_raw_buffer_load_b64(r, v, s, 16); }
DEV u32 ld32s(rsrc_t r, u32 v, u32 s) { return __builtin_amdgcn_raw_buffer_load_b32(r, v, s, 16); }
DEV void st128s(u32x4 d, rsrc_t r, u32 v, u32 s) { __builtin_amdgcn_raw_buffer_store_b128(d, r, v, s, 16); }
DEV void st64s(u32x2 d, rsrc_t r, u32 v, u32 s) { __builtin_amdgcn_raw_buffer_store_b64(d, r, v, s, 16); }
DEV void st32s(u32 d, rsrc_t r, u32 v, u32 s) { __builtin_amdgcn_raw_buffer_store_b32(d, r, v, s, 16); }

#define OPAQUE(x) asm volatile("" : "+v"(x))

struct __attribute__((aligned(16))) Lds {
  u32 ys[320];
  u32 xs[320];
  u32 x1s[320];
  u32 qs[64];
  float qpart[4][16];
  float mw[2][8];
  float part[8][132];
  float wpart[8][32];
  u32 x1m[8];
  u32 tag;
  u32 f_y, f_q, f_ep, c_q, c_part, c_role, c_x1;
};

DEV void lds_wait(u32* f, u32 v) {
  int sp = 0;
  while (true) {
    CBAR();
    u32 t = __builtin_amdgcn_readfirstlane(*f);
    if (t >= v || ++sp > SPIN_L) break;
  }
  CBAR();
}
typedef __attribute__((address_space(3))) u32 lds_u32;
DEV u32 lds_inc(u32* c) {
  u32 r;
  const u32 addr = (u32)(size_t)(lds_u32*)c;
  asm volatile("s_mov_b64 exec, 1\n\tds_add_rtn_u32 %0, %1, %2\n\ts_mov_b64 exec, -1\n\ts_waitcnt lgkmcnt(0)"
               : "=&v"(r) : "v"(addr), "v"(1u) : "memory");
  return __builtin_amdgcn_readfirstlane(r);
}
DEV void lds_inc_nr(u32* c) {
  const u32 addr = (u32)(size_t)(lds_u32*)c;
  asm volatile("s_mov_b64 exec, 1\n\tds_add_u32 %0, %1\n\ts_mov_b64 exec, -1" :: "v"(addr), "v"(1u) : "memory");
}
#pragma float_control(precise, off, push)
DEV float fmx(float a, float b) { return __builtin_fmaxf(a, b); }
#pragma float_control(pop)

#define LN_PASS(p0, p1, p2, p3, p4) do { \
    float s_ = dot2(p0, 0x3f803f80u, 0.f), ss_ = dot2(p0, p0, 0.f); \
    s_ = dot2(p1, 0x3f803f80u, s_); ss_ = dot2(p1, p1, ss_); \
    s_ = dot2(p2, 0x3f803f80u, s_); ss_ = dot2(p2, p2, ss_); \
    s_ = dot2(p3, 0x3f803f80u, s_); ss_ = dot2(p3, p3, ss_); \
    s_ = dot2(p4, 0x3f803f80u, s_); ss_ = dot2(p4, p4, ss_); \
    float t_ = red32(s_, ss_); \
    t_ += dppf<QP_X1>(t_); t_ += dppf<QP_X2>(t_); t_ += dppf<ROR4>(t_); t_ += dppf<ROR8>(t_); \
    const float S_ = rdlane(t_, 0) + rdlane(t_, 16), SS_ = rdlane(t_, 32) + rdlane(t_, 48); \
    const float mean_ = S_ * (1.f / 640.f); \
    const float var_ = fmx(__builtin_fmaf(-mean_, mean_, SS_ * (1.f / 640.f)), 0.f); \
    const float m_ = rb(mean_); \
    const float r_ = __builtin_amdgcn_rcpf(rb(__builtin_amdgcn_sqrtf(rb(rb(var_) + 1e-5f)))); \
    p0 = pk(rb(lo2f(p0) - m_) * r_, rb(hi2f(p0) - m_) * r_); \
    p1 = pk(rb(lo2f(p1) - m_) * r_, rb(hi2f(p1) - m_) * r_); \
    p2 = pk(rb(lo2f(p2) - m_) * r_, rb(hi2f(p2) - m_) * r_); \
    p3 = pk(rb(lo2f(p3) - m_) * r_, rb(hi2f(p3) - m_) * r_); \
    p4 = pk(rb(lo2f(p4) - m_) * r_, rb(hi2f(p4) - m_) * r_); \
  } while (0)

#define KLOAD(vo, base) asm volatile( \
  "global_load_dwordx4 v[128:131], %0, %1 offset:-4096 nt\n\t" \
  "global_load_dwordx4 v[132:135], %0, %1 offset:-3968 nt\n\t" \
  "global_load_dwordx4 v[136:139], %0, %1 offset:-3072 nt\n\t" \
  "global_load_dwordx4 v[140:143], %0, %1 offset:-2944 nt\n\t" \
  "global_load_dwordx4 v[144:147], %0, %1 offset:-2048 nt\n\t" \
  "global_load_dwordx4 v[148:151], %0, %1 offset:-1920 nt\n\t" \
  "global_load_dwordx4 v[152:155], %0, %1 offset:-1024 nt\n\t" \
  "global_load_dwordx4 v[156:159], %0, %1 offset:-896 nt\n\t" \
  "global_load_dwordx4 v[160:163], %0, %1 offset:0 nt\n\t" \
  "global_load_dwordx4 v[164:167], %0, %1 offset:128 nt\n\t" \
  "global_load_dwordx4 v[168:171], %0, %1 offset:1024 nt\n\t" \
  "global_load_dwordx4 v[172:175], %0, %1 offset:1152 nt\n\t" \
  "global_load_dwordx4 v[176:179], %0, %1 offset:2048 nt\n\t" \
  "global_load_dwordx4 v[180:183], %0, %1 offset:2176 nt\n\t" \
  "global_load_dwordx4 v[184:187], %0, %1 offset:3072 nt\n\t" \
  "global_load_dwordx4 v[188:191], %0, %1 offset:3200 nt\n\t" \
  :: "v"(vo), "s"(base) : "memory", "v255")
#define VLOAD(vo, b0, b1) asm volatile( \
  "global_load_dwordx4 v[192:195], %0, %1 offset:-4096 nt\n\t" \
  "global_load_dwordx4 v[196:199], %0, %1 offset:-3072 nt\n\t" \
  "global_load_dwordx4 v[200:203], %0, %1 offset:-2048 nt\n\t" \
  "global_load_dwordx4 v[204:207], %0, %1 offset:-1024 nt\n\t" \
  "global_load_dwordx4 v[208:211], %0, %1 offset:0 nt\n\t" \
  "global_load_dwordx4 v[212:215], %0, %1 offset:1024 nt\n\t" \
  "global_load_dwordx4 v[216:219], %0, %1 offset:2048 nt\n\t" \
  "global_load_dwordx4 v[220:223], %0, %1 offset:3072 nt\n\t" \
  "global_load_dwordx4 v[224:227], %0, %2 offset:-4096 nt\n\t" \
  "global_load_dwordx4 v[228:231], %0, %2 offset:-3072 nt\n\t" \
  "global_load_dwordx4 v[232:235], %0, %2 offset:-2048 nt\n\t" \
  "global_load_dwordx4 v[236:239], %0, %2 offset:-1024 nt\n\t" \
  "global_load_dwordx4 v[240:243], %0, %2 offset:0 nt\n\t" \
  "global_load_dwordx4 v[244:247], %0, %2 offset:1024 nt\n\t" \
  "global_load_dwordx4 v[248:251], %0, %2 offset:2048 nt\n\t" \
  "global_load_dwordx4 v[252:255], %0, %2 offset:3072 nt\n\t" \
  :: "v"(vo), "s"(b0), "s"(b1) : "memory", "v255")
#define WQLOAD(o0, o1, base) asm volatile( \
  "global_load_dwordx2 v[192:193], %0, %2\n\t" \
  "v_add_u32 %0, 0x2000, %0\n\t" \
  "global_load_dwordx2 v[194:195], %0, %2\n\t" \
  "v_add_u32 %0, 0x2000, %0\n\t" \
  "global_load_dwordx2 v[196:197], %0, %2\n\t" \
  "v_add_u32 %0, 0x2000, %0\n\t" \
  "global_load_dwordx2 v[198:199], %0, %2\n\t" \
  "v_add_u32 %0, 0x2000, %0\n\t" \
  "global_load_dwordx2 v[200:201], %0, %2\n\t" \
  "v_add_u32 %0, 0x2000, %0\n\t" \
  "global_load_dwordx2 v[202:203], %0, %2\n\t" \
  "v_add_u32 %0, 0x2000, %0\n\t" \
  "global_load_dwordx2 v[204:205], %0, %2\n\t" \
  "v_add_u32 %0, 0x2000, %0\n\t" \
  "global_load_dwordx2 v[206:207], %0, %2\n\t" \
  "global_load_dwordx2 v[208:209], %1, %2\n\t" \
  "v_add_u32 %1, 0x2000, %1\n\t" \
  "global_load_dwordx2 v[210:211], %1, %2\n\t" \
  : "+v"(o0), "+v"(o1) : "s"(base) : "memory", "v255")
#define WQPERM() asm volatile( \
  "s_waitcnt vmcnt(16)\n\t" \
  "v_perm_b32 v68, v194, v192, %0\n\t" \
  "v_perm_b32 v69, v194, v192, %1\n\t" \
  "v_perm_b32 v70, v195, v193, %0\n\t" \
  "v_perm_b32 v71, v195, v193, %1\n\t" \
  "v_perm_b32 v72, v198, v196, %0\n\t" \
  "v_perm_b32 v73, v198, v196, %1\n\t" \
  "v_perm_b32 v74, v199, v197, %0\n\t" \
  "v_perm_b32 v75, v199, v197, %1\n\t" \
  "v_perm_b32 v76, v202, v200, %0\n\t" \
  "v_perm_b32 v77, v202, v200, %1\n\t" \
  "v_perm_b32 v78, v203, v201, %0\n\t" \
  "v_perm_b32 v79, v203, v201, %1\n\t" \
  "v_perm_b32 v80, v206, v204, %0\n\t" \
  "v_perm_b32 v81, v206, v204, %1\n\t" \
  "v_perm_b32 v82, v207, v205, %0\n\t" \
  "v_perm_b32 v83, v207, v205, %1\n\t" \
  "v_perm_b32 v84, v210, v208, %0\n\t" \
  "v_perm_b32 v85, v210, v208, %1\n\t" \
  "v_perm_b32 v86, v211, v209, %0\n\t" \
  "v_perm_b32 v87, v211, v209, %1\n\t" \
  :: "s"(0x05040100u), "s"(0x07060302u) : "memory")
#define QGEMV(a0, a1, a2, a3, y0, y1, y2, y3, y4) asm volatile( \
  "v_dot2_f32_bf16 %0, v68, %4, 0\n\t" \
  "v_dot2_f32_bf16 %1, v69, %4, 0\n\t" \
  "v_dot2_f32_bf16 %2, v70, %4, 0\n\t" \
  "v_dot2_f32_bf16 %3, v71, %4, 0\n\t" \
  "v_dot2_f32_bf16 %0, v72, %5, %0\n\t" \
  "v_dot2_f32_bf16 %1, v73, %5, %1\n\t" \
  "v_dot2_f32_bf16 %2, v74, %5, %2\n\t" \
  "v_dot2_f32_bf16 %3, v75, %5, %3\n\t" \
  "v_dot2_f32_bf16 %0, v76, %6, %0\n\t" \
  "v_dot2_f32_bf16 %1, v77, %6, %1\n\t" \
  "v_dot2_f32_bf16 %2, v78, %6, %2\n\t" \
  "v_dot2_f32_bf16 %3, v79, %6, %3\n\t" \
  "v_dot2_f32_bf16 %0, v80, %7, %0\n\t" \
  "v_dot2_f32_bf16 %1, v81, %7, %1\n\t" \
  "v_dot2_f32_bf16 %2, v82, %7, %2\n\t" \
  "v_dot2_f32_bf16 %3, v83, %7, %3\n\t" \
  "v_dot2_f32_bf16 %0, v84, %8, %0\n\t" \
  "v_dot2_f32_bf16 %1, v85, %8, %1\n\t" \
  "v_dot2_f32_bf16 %2, v86, %8, %2\n\t" \
  "v_dot2_f32_bf16 %3, v87, %8, %3\n\t" \
  "s_nop 2\n\t" \
  : "=&v"(a0), "=&v"(a1), "=&v"(a2), "=&v"(a3) : "v"(y0), "v"(y1), "v"(y2), "v"(y3), "v"(y4) : "memory")
#define QK() asm volatile( \
  "s_waitcnt vmcnt(30)\n\t" \
  "v_dot2_f32_bf16 %0, v128, %8, 0\n\t" \
  "v_dot2_f32_bf16 %0, v129, %9, %0\n\t" \
  "v_dot2_f32_bf16 %0, v130, %10, %0\n\t" \
  "v_dot2_f32_bf16 %0, v131, %11, %0\n\t" \
  "v_dot2_f32_bf16 %0, v132, %12, %0\n\t" \
  "v_dot2_f32_bf16 %0, v133, %13, %0\n\t" \
  "v_dot2_f32_bf16 %0, v134, %14, %0\n\t" \
  "v_dot2_f32_bf16 %0, v135, %15, %0\n\t" \
  "s_waitcnt vmcnt(28)\n\t" \
  "v_dot2_f32_bf16 %1, v136, %8, 0\n\t" \
  "v_dot2_f32_bf16 %1, v137, %9, %1\n\t" \
  "v_dot2_f32_bf16 %1, v138, %10, %1\n\t" \
  "v_dot2_f32_bf16 %1, v139, %11, %1\n\t" \
  "v_dot2_f32_bf16 %1, v140, %12, %1\n\t" \
  "v_dot2_f32_bf16 %1, v141, %13, %1\n\t" \
  "v_dot2_f32_bf16 %1, v142, %14, %1\n\t" \
  "v_dot2_f32_bf16 %1, v143, %15, %1\n\t" \
  "s_waitcnt vmcnt(26)\n\t" \
  "v_dot2_f32_bf16 %2, v144, %8, 0\n\t" \
  "v_dot2_f32_bf16 %2, v145, %9, %2\n\t" \
  "v_dot2_f32_bf16 %2, v146, %10, %2\n\t" \
  "v_dot2_f32_bf16 %2, v147, %11, %2\n\t" \
  "v_dot2_f32_bf16 %2, v148, %12, %2\n\t" \
  "v_dot2_f32_bf16 %2, v149, %13, %2\n\t" \
  "v_dot2_f32_bf16 %2, v150, %14, %2\n\t" \
  "v_dot2_f32_bf16 %2, v151, %15, %2\n\t" \
  "s_waitcnt vmcnt(24)\n\t" \
  "v_dot2_f32_bf16 %3, v152, %8, 0\n\t" \
  "v_dot2_f32_bf16 %3, v153, %9, %3\n\t" \
  "v_dot2_f32_bf16 %3, v154, %10, %3\n\t" \
  "v_dot2_f32_bf16 %3, v155, %11, %3\n\t" \
  "v_dot2_f32_bf16 %3, v156, %12, %3\n\t" \
  "v_dot2_f32_bf16 %3, v157, %13, %3\n\t" \
  "v_dot2_f32_bf16 %3, v158, %14, %3\n\t" \
  "v_dot2_f32_bf16 %3, v159, %15, %3\n\t" \
  "s_waitcnt vmcnt(22)\n\t" \
  "v_dot2_f32_bf16 %4, v160, %8, 0\n\t" \
  "v_dot2_f32_bf16 %4, v161, %9, %4\n\t" \
  "v_dot2_f32_bf16 %4, v162, %10, %4\n\t" \
  "v_dot2_f32_bf16 %4, v163, %11, %4\n\t" \
  "v_dot2_f32_bf16 %4, v164, %12, %4\n\t" \
  "v_dot2_f32_bf16 %4, v165, %13, %4\n\t" \
  "v_dot2_f32_bf16 %4, v166, %14, %4\n\t" \
  "v_dot2_f32_bf16 %4, v167, %15, %4\n\t" \
  "s_waitcnt vmcnt(20)\n\t" \
  "v_dot2_f32_bf16 %5, v168, %8, 0\n\t" \
  "v_dot2_f32_bf16 %5, v169, %9, %5\n\t" \
  "v_dot2_f32_bf16 %5, v170, %10, %5\n\t" \
  "v_dot2_f32_bf16 %5, v171, %11, %5\n\t" \
  "v_dot2_f32_bf16 %5, v172, %12, %5\n\t" \
  "v_dot2_f32_bf16 %5, v173, %13, %5\n\t" \
  "v_dot2_f32_bf16 %5, v174, %14, %5\n\t" \
  "v_dot2_f32_bf16 %5, v175, %15, %5\n\t" \
  "s_waitcnt vmcnt(18)\n\t" \
  "v_dot2_f32_bf16 %6, v176, %8, 0\n\t" \
  "v_dot2_f32_bf16 %6, v177, %9, %6\n\t" \
  "v_dot2_f32_bf16 %6, v178, %10, %6\n\t" \
  "v_dot2_f32_bf16 %6, v179, %11, %6\n\t" \
  "v_dot2_f32_bf16 %6, v180, %12, %6\n\t" \
  "v_dot2_f32_bf16 %6, v181, %13, %6\n\t" \
  "v_dot2_f32_bf16 %6, v182, %14, %6\n\t" \
  "v_dot2_f32_bf16 %6, v183, %15, %6\n\t" \
  "s_waitcnt vmcnt(16)\n\t" \
  "v_dot2_f32_bf16 %7, v184, %8, 0\n\t" \
  "v_dot2_f32_bf16 %7, v185, %9, %7\n\t" \
  "v_dot2_f32_bf16 %7, v186, %10, %7\n\t" \
  "v_dot2_f32_bf16 %7, v187, %11, %7\n\t" \
  "v_dot2_f32_bf16 %7, v188, %12, %7\n\t" \
  "v_dot2_f32_bf16 %7, v189, %13, %7\n\t" \
  "v_dot2_f32_bf16 %7, v190, %14, %7\n\t" \
  "v_dot2_f32_bf16 %7, v191, %15, %7\n\t" \
  "s_nop 2\n\t" \
  "v_add_f32_dpp %0, %0, %0 row_half_mirror row_mask:0xf bank_mask:0x5\n\t" \
  "v_add_f32_dpp %1, %1, %1 row_half_mirror row_mask:0xf bank_mask:0x5\n\t" \
  "v_add_f32_dpp %2, %2, %2 row_half_mirror row_mask:0xf bank_mask:0x5\n\t" \
  "v_add_f32_dpp %3, %3, %3 row_half_mirror row_mask:0xf bank_mask:0x5\n\t" \
  "v_add_f32_dpp %0, %4, %4 row_half_mirror row_mask:0xf bank_mask:0xa\n\t" \
  "v_add_f32_dpp %1, %5, %5 row_half_mirror row_mask:0xf bank_mask:0xa\n\t" \
  "v_add_f32_dpp %2, %6, %6 row_half_mirror row_mask:0xf bank_mask:0xa\n\t" \
  "v_add_f32_dpp %3, %7, %7 row_half_mirror row_mask:0xf bank_mask:0xa\n\t" \
  "s_nop 1\n\t" \
  : "=&v"(a0), "=&v"(a1), "=&v"(a2), "=&v"(a3), "=&v"(a4), "=&v"(a5), "=&v"(a6), "=&v"(a7) : "v"(q0), "v"(q1), "v"(q2), "v"(q3), "v"(q4), "v"(q5), "v"(q6), "v"(q7) : "memory")
#define PVALL(o0, o1, X, L) asm volatile( \
  "v_mov_b32_dpp v124, %2 row_ror:2 row_mask:0xf bank_mask:0x1\n\t" \
  "v_mov_b32_dpp v125, %2 row_ror:6 row_mask:0xf bank_mask:0x2\n\t" \
  "v_mov_b32_dpp v126, %2 row_ror:10 row_mask:0xf bank_mask:0x4\n\t" \
  "v_mov_b32_dpp v127, %2 row_ror:14 row_mask:0xf bank_mask:0x8\n\t" \
  "v_mov_b32_dpp v124, %2 row_ror:2 row_mask:0xf bank_mask:0x1\n\t" \
  "v_mov_b32_dpp v125, %2 row_ror:6 row_mask:0xf bank_mask:0x2\n\t" \
  "v_mfma_f32_16x16x32_bf16 v[56:59], v[120:123], v[240:243], v[56:59]\n\t" \
  "v_mov_b32_dpp v126, %2 row_ror:10 row_mask:0xf bank_mask:0x4\n\t" \
  "v_mov_b32_dpp v127, %2 row_ror:14 row_mask:0xf bank_mask:0x8\n\t" \
  "v_mfma_f32_16x16x32_bf16 v[60:63], v[120:123], v[244:247], v[60:63]\n\t" \
  "s_cbranch_scc1 2f\n\t" \
  "s_waitcnt vmcnt(0)\n\t" \
  "v_perm_b32 v64, v252, v248, %4\n\t" \
  "v_perm_b32 v252, v252, v248, %5\n\t" \
  "v_perm_b32 v65, v253, v249, %4\n\t" \
  "v_perm_b32 v253, v253, v249, %5\n\t" \
  "v_perm_b32 v66, v254, v250, %4\n\t" \
  "v_perm_b32 v254, v254, v250, %5\n\t" \
  "v_perm_b32 v67, v255, v251, %4\n\t" \
  "v_perm_b32 v255, v255, v251, %5\n\t" \
  "v_mov_b32 v248, v64\n\t" \
  "v_mov_b32 v249, v65\n\t" \
  "v_mov_b32 v250, v66\n\t" \
  "v_mov_b32 v251, v67\n\t" \
  "2:\n\t" \
  "s_nop 1\n\t" \
  "v_mfma_f32_16x16x32_bf16 v[56:59], v[124:127], v[248:251], v[56:59]\n\t" \
  "v_mfma_f32_16x16x32_bf16 v[60:63], v[124:127], v[252:255], v[60:63]\n\t" \
  "s_nop 7\n\t" \
  "v_add_f32 %0, v56, v57\n\t" \
  "v_add_f32 %1, v60, v61\n\t" \
  : "=&v"(o0), "=&v"(o1) : "v"(X), "s"(L), "s"(0x05040100u), "s"(0x07060302u) : "memory", "scc", "v56", "v57", "v58", "v59", "v60", "v61", "v62", "v63")
#define AZERO() asm volatile( \
  "v_mov_b32 v120, 0\n\t" \
  "v_mov_b32 v121, 0\n\t" \
  "v_mov_b32 v122, 0\n\t" \
  "v_mov_b32 v123, 0\n\t" \
  "v_mov_b32 v124, 0\n\t" \
  "v_mov_b32 v125, 0\n\t" \
  "v_mov_b32 v126, 0\n\t" \
  "v_mov_b32 v127, 0\n\t" \
  ::: "memory")
#define RWLOAD_W(vo, base) asm volatile( \
  "global_load_dwordx4 v[88:91], %0, %1 offset:0\n\t" \
  "global_load_dwordx4 v[92:95], %0, %1 offset:1280\n\t" \
  "global_load_dwordx4 v[96:99], %0, %1 offset:2560\n\t" \
  "global_load_dwordx4 v[100:103], %0, %1 offset:3840\n\t" \
  : "+v"(vo) : "s"(base) : "memory", "v255")
#define RWLOAD_M1A(vo, base) asm volatile( \
  "global_load_dwordx4 v[88:91], %0, %1 offset:0\n\t" \
  "global_load_dwordx4 v[92:95], %0, %1 offset:3456\n\t" \
  "v_add_u32 %0, 0x1b00, %0\n\t" \
  "global_load_dwordx4 v[96:99], %0, %1 offset:0\n\t" \
  "global_load_dwordx4 v[100:103], %0, %1 offset:3456\n\t" \
  "v_add_u32 %0, 0x1b00, %0\n\t" \
  "global_load_dwordx4 v[104:107], %0, %1 offset:0\n\t" \
  "global_load_dwordx4 v[108:111], %0, %1 offset:3456\n\t" \
  "v_add_u32 %0, 0x1b00, %0\n\t" \
  "global_load_dwordx4 v[112:115], %0, %1 offset:0\n\t" \
  "global_load_dwordx4 v[116:119], %0, %1 offset:3456\n\t" \
  : "+v"(vo) : "s"(base) : "memory", "v255")
#define RWLOAD_M1B(vo, base) asm volatile( \
  "global_load_dwordx4 v[80:83], %0, %1\n\t" \
  "global_load_dwordx4 v[84:87], %0, %1 offset:3456\n\t" \
  :: "v"(vo), "s"(base) : "memory", "v255")
#define RWLOAD_M2(vo, base) asm volatile( \
  "global_load_dwordx4 v[88:91], %0, %1 offset:0\n\t" \
  "global_load_dwordx4 v[92:95], %0, %1 offset:1280\n\t" \
  "global_load_dwordx4 v[96:99], %0, %1 offset:2560\n\t" \
  "global_load_dwordx4 v[100:103], %0, %1 offset:3840\n\t" \
  "v_add_u32 %0, 0x1400, %0\n\t" \
  "global_load_dwordx4 v[104:107], %0, %1 offset:0\n\t" \
  "global_load_dwordx4 v[108:111], %0, %1 offset:1280\n\t" \
  "global_load_dwordx4 v[112:115], %0, %1 offset:2560\n\t" \
  "global_load_dwordx4 v[116:119], %0, %1 offset:3840\n\t" \
  : "+v"(vo) : "s"(base) : "memory", "v255")
#define RWPERM4() asm volatile( \
  "s_waitcnt vmcnt(0)\n\t" \
  "v_perm_b32 v64, v92, v88, %0\n\t" \
  "v_perm_b32 v92, v92, v88, %1\n\t" \
  "v_perm_b32 v65, v93, v89, %0\n\t" \
  "v_perm_b32 v93, v93, v89, %1\n\t" \
  "v_perm_b32 v66, v94, v90, %0\n\t" \
  "v_perm_b32 v94, v94, v90, %1\n\t" \
  "v_perm_b32 v67, v95, v91, %0\n\t" \
  "v_perm_b32 v95, v95, v91, %1\n\t" \
  "v_mov_b32 v88, v64\n\t" \
  "v_mov_b32 v89, v65\n\t" \
  "v_mov_b32 v90, v66\n\t" \
  "v_mov_b32 v91, v67\n\t" \
  "v_perm_b32 v64, v100, v96, %0\n\t" \
  "v_perm_b32 v100, v100, v96, %1\n\t" \
  "v_perm_b32 v65, v101, v97, %0\n\t" \
  "v_perm_b32 v101, v101, v97, %1\n\t" \
  "v_perm_b32 v66, v102, v98, %0\n\t" \
  "v_perm_b32 v102, v102, v98, %1\n\t" \
  "v_perm_b32 v67, v103, v99, %0\n\t" \
  "v_perm_b32 v103, v103, v99, %1\n\t" \
  "v_mov_b32 v96, v64\n\t" \
  "v_mov_b32 v97, v65\n\t" \
  "v_mov_b32 v98, v66\n\t" \
  "v_mov_b32 v99, v67\n\t" \
  "v_perm_b32 v64, v108, v104, %0\n\t" \
  "v_perm_b32 v108, v108, v104, %1\n\t" \
  "v_perm_b32 v65, v109, v105, %0\n\t" \
  "v_perm_b32 v109, v109, v105, %1\n\t" \
  "v_perm_b32 v66, v110, v106, %0\n\t" \
  "v_perm_b32 v110, v110, v106, %1\n\t" \
  "v_perm_b32 v67, v111, v107, %0\n\t" \
  "v_perm_b32 v111, v111, v107, %1\n\t" \
  "v_mov_b32 v104, v64\n\t" \
  "v_mov_b32 v105, v65\n\t" \
  "v_mov_b32 v106, v66\n\t" \
  "v_mov_b32 v107, v67\n\t" \
  "v_perm_b32 v64, v116, v112, %0\n\t" \
  "v_perm_b32 v116, v116, v112, %1\n\t" \
  "v_perm_b32 v65, v117, v113, %0\n\t" \
  "v_perm_b32 v117, v117, v113, %1\n\t" \
  "v_perm_b32 v66, v118, v114, %0\n\t" \
  "v_perm_b32 v118, v118, v114, %1\n\t" \
  "v_perm_b32 v67, v119, v115, %0\n\t" \
  "v_perm_b32 v119, v119, v115, %1\n\t" \
  "v_mov_b32 v112, v64\n\t" \
  "v_mov_b32 v113, v65\n\t" \
  "v_mov_b32 v114, v66\n\t" \
  "v_mov_b32 v115, v67\n\t" \
  :: "s"(0x05040100u), "s"(0x07060302u) : "memory")
#define RWPERM_T() asm volatile( \
  "s_waitcnt vmcnt(0)\n\t" \
  "v_perm_b32 v64, v84, v80, %0\n\t" \
  "v_perm_b32 v84, v84, v80, %1\n\t" \
  "v_perm_b32 v65, v85, v81, %0\n\t" \
  "v_perm_b32 v85, v85, v81, %1\n\t" \
  "v_perm_b32 v66, v86, v82, %0\n\t" \
  "v_perm_b32 v86, v86, v82, %1\n\t" \
  "v_perm_b32 v67, v87, v83, %0\n\t" \
  "v_perm_b32 v87, v87, v83, %1\n\t" \
  "v_mov_b32 v80, v64\n\t" \
  "v_mov_b32 v81, v65\n\t" \
  "v_mov_b32 v82, v66\n\t" \
  "v_mov_b32 v83, v67\n\t" \
  :: "s"(0x05040100u), "s"(0x07060302u) : "memory")
#define RGEMV2(y0, y1) asm volatile( \
  "v_dot2_f32_bf16 %0, v88, %8, 0\n\t" \
  "v_dot2_f32_bf16 %1, v92, %8, 0\n\t" \
  "v_dot2_f32_bf16 %2, v89, %8, 0\n\t" \
  "v_dot2_f32_bf16 %3, v93, %8, 0\n\t" \
  "v_dot2_f32_bf16 %4, v90, %8, 0\n\t" \
  "v_dot2_f32_bf16 %5, v94, %8, 0\n\t" \
  "v_dot2_f32_bf16 %6, v91, %8, 0\n\t" \
  "v_dot2_f32_bf16 %7, v95, %8, 0\n\t" \
  "v_dot2_f32_bf16 %0, v96, %9, %0\n\t" \
  "v_dot2_f32_bf16 %1, v100, %9, %1\n\t" \
  "v_dot2_f32_bf16 %2, v97, %9, %2\n\t" \
  "v_dot2_f32_bf16 %3, v101, %9, %3\n\t" \
  "v_dot2_f32_bf16 %4, v98, %9, %4\n\t" \
  "v_dot2_f32_bf16 %5, v102, %9, %5\n\t" \
  "v_dot2_f32_bf16 %6, v99, %9, %6\n\t" \
  "v_dot2_f32_bf16 %7, v103, %9, %7\n\t" \
  "s_nop 2\n\t" \
  : "=&v"(a0), "=&v"(a1), "=&v"(a2), "=&v"(a3), "=&v"(a4), "=&v"(a5), "=&v"(a6), "=&v"(a7) : "v"(y0), "v"(y1) : "memory")
#define RGEMV4(y0, y1, y2, y3) asm volatile( \
  "v_dot2_f32_bf16 %0, v88, %8, 0\n\t" \
  "v_dot2_f32_bf16 %1, v92, %8, 0\n\t" \
  "v_dot2_f32_bf16 %2, v89, %8, 0\n\t" \
  "v_dot2_f32_bf16 %3, v93, %8, 0\n\t" \
  "v_dot2_f32_bf16 %4, v90, %8, 0\n\t" \
  "v_dot2_f32_bf16 %5, v94, %8, 0\n\t" \
  "v_dot2_f32_bf16 %6, v91, %8, 0\n\t" \
  "v_dot2_f32_bf16 %7, v95, %8, 0\n\t" \
  "v_dot2_f32_bf16 %0, v96, %9, %0\n\t" \
  "v_dot2_f32_bf16 %1, v100, %9, %1\n\t" \
  "v_dot2_f32_bf16 %2, v97, %9, %2\n\t" \
  "v_dot2_f32_bf16 %3, v101, %9, %3\n\t" \
  "v_dot2_f32_bf16 %4, v98, %9, %4\n\t" \
  "v_dot2_f32_bf16 %5, v102, %9, %5\n\t" \
  "v_dot2_f32_bf16 %6, v99, %9, %6\n\t" \
  "v_dot2_f32_bf16 %7, v103, %9, %7\n\t" \
  "v_dot2_f32_bf16 %0, v104, %10, %0\n\t" \
  "v_dot2_f32_bf16 %1, v108, %10, %1\n\t" \
  "v_dot2_f32_bf16 %2, v105, %10, %2\n\t" \
  "v_dot2_f32_bf16 %3, v109, %10, %3\n\t" \
  "v_dot2_f32_bf16 %4, v106, %10, %4\n\t" \
  "v_dot2_f32_bf16 %5, v110, %10, %5\n\t" \
  "v_dot2_f32_bf16 %6, v107, %10, %6\n\t" \
  "v_dot2_f32_bf16 %7, v111, %10, %7\n\t" \
  "v_dot2_f32_bf16 %0, v112, %11, %0\n\t" \
  "v_dot2_f32_bf16 %1, v116, %11, %1\n\t" \
  "v_dot2_f32_bf16 %2, v113, %11, %2\n\t" \
  "v_dot2_f32_bf16 %3, v117, %11, %3\n\t" \
  "v_dot2_f32_bf16 %4, v114, %11, %4\n\t" \
  "v_dot2_f32_bf16 %5, v118, %11, %5\n\t" \
  "v_dot2_f32_bf16 %6, v115, %11, %6\n\t" \
  "v_dot2_f32_bf16 %7, v119, %11, %7\n\t" \
  "s_nop 2\n\t" \
  : "=&v"(a0), "=&v"(a1), "=&v"(a2), "=&v"(a3), "=&v"(a4), "=&v"(a5), "=&v"(a6), "=&v"(a7) : "v"(y0), "v"(y1), "v"(y2), "v"(y3) : "memory")
#define RGEMV5(y0, y1, y2, y3, y4) asm volatile( \
  "v_dot2_f32_bf16 %0, v88, %8, 0\n\t" \
  "v_dot2_f32_bf16 %1, v92, %8, 0\n\t" \
  "v_dot2_f32_bf16 %2, v89, %8, 0\n\t" \
  "v_dot2_f32_bf16 %3, v93, %8, 0\n\t" \
  "v_dot2_f32_bf16 %4, v90, %8, 0\n\t" \
  "v_dot2_f32_bf16 %5, v94, %8, 0\n\t" \
  "v_dot2_f32_bf16 %6, v91, %8, 0\n\t" \
  "v_dot2_f32_bf16 %7, v95, %8, 0\n\t" \
  "v_dot2_f32_bf16 %0, v96, %9, %0\n\t" \
  "v_dot2_f32_bf16 %1, v100, %9, %1\n\t" \
  "v_dot2_f32_bf16 %2, v97, %9, %2\n\t" \
  "v_dot2_f32_bf16 %3, v101, %9, %3\n\t" \
  "v_dot2_f32_bf16 %4, v98, %9, %4\n\t" \
  "v_dot2_f32_bf16 %5, v102, %9, %5\n\t" \
  "v_dot2_f32_bf16 %6, v99, %9, %6\n\t" \
  "v_dot2_f32_bf16 %7, v103, %9, %7\n\t" \
  "v_dot2_f32_bf16 %0, v104, %10, %0\n\t" \
  "v_dot2_f32_bf16 %1, v108, %10, %1\n\t" \
  "v_dot2_f32_bf16 %2, v105, %10, %2\n\t" \
  "v_dot2_f32_bf16 %3, v109, %10, %3\n\t" \
  "v_dot2_f32_bf16 %4, v106, %10, %4\n\t" \
  "v_dot2_f32_bf16 %5, v110, %10, %5\n\t" \
  "v_dot2_f32_bf16 %6, v107, %10, %6\n\t" \
  "v_dot2_f32_bf16 %7, v111, %10, %7\n\t" \
  "v_dot2_f32_bf16 %0, v112, %11, %0\n\t" \
  "v_dot2_f32_bf16 %1, v116, %11, %1\n\t" \
  "v_dot2_f32_bf16 %2, v113, %11, %2\n\t" \
  "v_dot2_f32_bf16 %3, v117, %11, %3\n\t" \
  "v_dot2_f32_bf16 %4, v114, %11, %4\n\t" \
  "v_dot2_f32_bf16 %5, v118, %11, %5\n\t" \
  "v_dot2_f32_bf16 %6, v115, %11, %6\n\t" \
  "v_dot2_f32_bf16 %7, v119, %11, %7\n\t" \
  "v_dot2_f32_bf16 %0, v80, %12, %0\n\t" \
  "v_dot2_f32_bf16 %1, v84, %12, %1\n\t" \
  "v_dot2_f32_bf16 %2, v81, %12, %2\n\t" \
  "v_dot2_f32_bf16 %3, v85, %12, %3\n\t" \
  "v_dot2_f32_bf16 %4, v82, %12, %4\n\t" \
  "v_dot2_f32_bf16 %5, v86, %12, %5\n\t" \
  "v_dot2_f32_bf16 %6, v83, %12, %6\n\t" \
  "v_dot2_f32_bf16 %7, v87, %12, %7\n\t" \
  "s_nop 2\n\t" \
  : "=&v"(a0), "=&v"(a1), "=&v"(a2), "=&v"(a3), "=&v"(a4), "=&v"(a5), "=&v"(a6), "=&v"(a7) : "v"(y0), "v"(y1), "v"(y2), "v"(y3), "v"(y4) : "memory")

#define STAMP(i) do {} while (0)

__global__ void __launch_bounds__(512, 2) __attribute__((amdgpu_num_vgpr(32)))
fused_kernel(const char* a0_, const char* a1_, const char* a2_, const char* a3_, const char* a4_, const char* a5_, const char* a6_, const char* a7_, const char* a8_, char* a9_, char* a10_) {
  __shared__ Lds S;
  u64 tstart;
  typedef u64 u64x8 __attribute__((ext_vector_type(8)));
  typedef u64 u64x2 __attribute__((ext_vector_type(2)));
  u64x8 ka; u64x2 kb; u64 kc;
  const void* kp = (const void*)__builtin_amdgcn_kernarg_segment_ptr();
  asm volatile("s_memrealtime %0\n\ts_waitcnt lgkmcnt(0)\n\ts_load_dwordx16 %1, %4, 0\n\ts_load_dwordx4 %2, %4, 0x40\n\ts_load_dwordx2 %3, %4, 0x50\n\ts_waitcnt lgkmcnt(0)\n\t" : "=&s"(tstart), "=&s"(ka), "=&s"(kb), "=&s"(kc) : "s"(kp) : "memory");
  const char* xin = (const char*)(const __attribute__((address_space(1))) char*)ka[0];
  const char* Kc = (const char*)(const __attribute__((address_space(1))) char*)ka[1];
  const char* Vc = (const char*)(const __attribute__((address_space(1))) char*)ka[2];
  const char* Wq = (const char*)(const __attribute__((address_space(1))) char*)ka[3];
  const char* Wo = (const char*)(const __attribute__((address_space(1))) char*)ka[4];
  const char* W1 = (const char*)(const __attribute__((address_space(1))) char*)ka[5];
  const char* B1 = (const char*)(const __attribute__((address_space(1))) char*)ka[6];
  const char* W2 = (const char*)(const __attribute__((address_space(1))) char*)ka[7];
  const char* B2 = (const char*)(const __attribute__((address_space(1))) char*)kb[0];
  char* out = (char*)(__attribute__((address_space(1))) char*)kb[1];
  char* ws = (char*)(__attribute__((address_space(1))) char*)kc;
  const int tid = threadIdx.x, lane0 = tid & 63;
  const int w = __builtin_amdgcn_readfirstlane(tid >> 6);
  const int b = blockIdx.x, g = b & 7, j = b >> 3, h = 4 * g + (j >> 3), s = j & 7;
  const int m1 = (j - 20) * 8 + g, m2 = g * 5 + (j - 27);
  const int role = j < 20 ? 0 : (j < 27 ? (m1 < 54 ? 1 : 3) : 2);
  const rsrc_t R = __builtin_amdgcn_make_buffer_rsrc(ws, 0, 0x200000, 0x00020000);
  const size_t kvoff = (size_t)(h * 8 + s) * 131072 + (size_t)w * 16384;
  u32 bias = 0;
  u32 x0 = 0, x1 = 0, x2 = 0, x3 = 0, x4 = 0, E = 0;

  {
    const int lane = lane0;
    if (w < 4) {
      const int sg = 16 * w + (lane >> 2);
      u32 o0 = 8 * sg * 8192 + (lane & 3) * 8, o1 = (512 + 2 * sg) * 8192 + (lane & 3) * 8;
      const char* wb = Wq + h * 256 + s * 32;
      WQLOAD(o0, o1, wb);
    } else if (w == 4) {
      __builtin_amdgcn_s_setprio(3);
      u32x4 xa = *(const u32x4*)(xin + lane * 16);
      x4 = *(const u32*)(xin + 1024 + lane * 4);
      E = ld32s(R, 0, WS_E);
      x0 = xa.x; x1 = xa.y; x2 = xa.z; x3 = xa.w;
    } else if (w == 7) {
      __builtin_amdgcn_s_setprio(3);
      const u32x4 ff = {SENT, SENT, SENT, SENT};
      u32 vo, so; bool act;
      if (role == 0) { vo = lane * 4; so = O_Z + g * 2560 + j * 128; act = lane < 32; }
      else if (role == 1) { vo = lane * 4; so = O_H + m1 * 128; act = lane < 16; }
      else { vo = (lane >> 3) * 8192 + (lane & 7) * 4; so = O_X + m2 * 128; act = role == 2; }
      if (lane < 33) {
        st128s(ff, R, lane * 16, WS_LB + O_REC + (h * 8 + s) * 640);
        st128s(ff, R, lane * 16, WS_LB + WS_LSTRIDE + O_REC + (h * 8 + s) * 640);
      }
      if (act) {
        st32s(SENT, R, vo, WS_LB + so);
        st32s(SENT, R, vo, WS_LB + WS_LSTRIDE + so);
      }
      __builtin_amdgcn_s_setprio(0);
    } else if (w == 5) {
      if (lane < 16) ((u32*)S.mw)[lane] = SENT;
      if (lane < 7) (&S.f_y)[lane] = 0;
    }
    AZERO();
    asm volatile("s_waitcnt lgkmcnt(0)\n\ts_barrier" ::: "memory");
    if (w < 4) {
      const u32 kvo = (32 * ((lane >> 3) & 1) + (lane >> 4)) * 256 + (lane & 7) * 16;
      const char* kb = Kc + kvoff + 4096;
      KLOAD(kvo, kb);
      __builtin_amdgcn_s_setprio(2);
    }
    if (w == 7) {
      asm volatile("s_waitcnt vmcnt(0)" ::: "memory");
      lds_wait(&S.f_y, 1);
      if (lane == 0) st32s(S.tag, R, 0, WS_FLAGS + b * 4);
    }
  }

#pragma unroll 1
  for (int L = 0; L < 2; L++) {
    const u32 lb = WS_LB + L * WS_LSTRIDE;
    if (w == 4) {
      int lane = lane0; OPAQUE(lane);
      if (L == 1) {
        const u32 xo = WS_LB + O_X + g * 8192;
        int sp = 0;
        while (true) {
          CBAR();
          u32x4 xa = ld128s(R, (lane >> 1) * 128 + (lane & 1) * 16, xo);
          x4 = ld32s(R, (32 + (lane >> 3)) * 128 + (lane & 7) * 4, xo);
          x0 = xa.x; x1 = xa.y; x2 = xa.z; x3 = xa.w;
          u32 mx = umax3(umax3(x0, x1, x2), x3, x4);
          if (__builtin_amdgcn_ballot_w64(mx == SENT) == 0 || ++sp > SPIN_G) break;
        }
      }
      STAMP(1);
      *(u32x4*)&S.xs[4 * lane] = u32x4{x0, x1, x2, x3};
      S.xs[256 + lane] = x4;
      LN_PASS(x0, x1, x2, x3, x4);
      *(u32x4*)&S.ys[4 * lane] = u32x4{x0, x1, x2, x3};
      S.ys[256 + lane] = x4;
      const u32 tag = E + 0x7F4A7C15u;
      S.tag = tag;
      CBAR();
      *(u32*)&S.f_y = L + 1;
      CBAR();
      STAMP(2);
      u32x2 q;
      int sp = 0;
      while (true) {
        CBAR();
        q = ld64s(R, (lane >> 3) * 128 + (lane & 7) * 8, lb + O_Q + h * 1024);
        if (__builtin_amdgcn_ballot_w64(q.y != tag) == 0 || ++sp > SPIN_G) break;
      }
      S.qs[lane] = q.x;
      CBAR();
      *(u32*)&S.f_q = L + 1;
      CBAR();
      __builtin_amdgcn_s_setprio(0);
      STAMP(4);
    } else if (w < 4) {
      int lane = lane0; OPAQUE(lane);
      if (L == 0) WQPERM();
      const int sg = 16 * w + (lane >> 2);
      lds_wait(&S.f_y, L + 1);
      u32x4 y4 = *(const u32x4*)&S.ys[4 * sg];
      u32 y0 = y4.x, y1 = y4.y, y2 = y4.z, y3 = y4.w, y5 = S.ys[256 + sg];
      float a0, a1, a2, a3;
      QGEMV(a0, a1, a2, a3, y0, y1, y2, y3, y5);
      float t = red16(red32(a0, a2), red32(a1, a3));
      t += dppf<ROR4>(t);
      t += dppf<ROR8>(t);
      if ((lane & 12) == 0) S.qpart[w][4 * (lane & 3) + (lane >> 4)] = t;
      u32 c = lds_inc(&S.c_q);
      if (c == 4 * L + 3) {
        if (lane < 8) {
          f32x2 q0 = *(const f32x2*)&S.qpart[0][2 * lane], q1 = *(const f32x2*)&S.qpart[1][2 * lane];
          f32x2 q2 = *(const f32x2*)&S.qpart[2][2 * lane], q3 = *(const f32x2*)&S.qpart[3][2 * lane];
          f32x2 q = (q0 + q1) + (q2 + q3);
          st64s(u32x2{pk(q.x, q.y), S.tag}, R, lane * 8, lb + O_Q + (h * 8 + s) * 128);
        }
        STAMP(3);
      }
      __builtin_amdgcn_s_setprio(0);
    }
    int lane = lane0; OPAQUE(lane);
    if (L == 0) {
      if (w >= 4) {
        const u32 kvo = (32 * ((lane >> 3) & 1) + (lane >> 4)) * 256 + (lane & 7) * 16;
        const char* kb = Kc + kvoff + 4096;
        KLOAD(kvo, kb);
      }
      const u32 vvo = lane * 16;
      const char* vb0 = Vc + kvoff + 4096;
      const char* vb1 = Vc + kvoff + 12288;
      VLOAD(vvo, vb0, vb1);
    }
    lds_wait(&S.f_q, L + 1);
    STAMP(5);
    float sc;
    {
      const int ch = lane & 7;
      u32x4 qa = *(const u32x4*)&S.qs[4 * ch], qb = *(const u32x4*)&S.qs[32 + 4 * ch];
      u32 q0 = qa.x, q1 = qa.y, q2 = qa.z, q3 = qa.w, q4 = qb.x, q5 = qb.y, q6 = qb.z, q7 = qb.w;
      float a0, a1, a2, a3, a4, a5, a6, a7;
      QK();
      const bool b1 = lane & 2, b0 = lane & 1;
      float t0 = (b1 ? a2 : a0) + dppf<QP_X2>(b1 ? a0 : a2);
      float t1 = (b1 ? a3 : a1) + dppf<QP_X2>(b1 ? a1 : a3);
      sc = (b0 ? t1 : t0) + dppf<QP_X1>(b0 ? t0 : t1);
      sc = rb(rb(sc) * 0.08838834764831845f);
    }
    float mx = fmx(sc, dppf<QP_X1>(sc));
    mx = fmx(mx, dppf<QP_X2>(mx));
    mx = fmx(mx, dppf<ROR4>(mx));
    mx = fmx(mx, dppf<ROR8>(mx));
    const float mwv = fmx(fmx(rdlane(mx, 0), rdlane(mx, 16)), fmx(rdlane(mx, 32), rdlane(mx, 48)));
    *(u32*)&S.mw[L][w] = clampS(f2u(mwv)); CBAR();
    const float p = __builtin_amdgcn_exp2f((sc - mwv) * LOG2E);
    u32 X;
    {
      const bool odd = lane & 1;
      float lo = p - hi2f(f2u(p));
      float own = odd ? lo : p, snd = odd ? p : lo;
      u32 rcv = dppu<QP_X1>(f2u(snd));
      X = __builtin_amdgcn_perm(rcv, f2u(own), odd ? 0x03020706u : 0x07060302u);
    }
    float ls = p + dppf<QP_X1>(p);
    ls += dppf<QP_X2>(ls);
    ls += dppf<ROR4>(ls);
    ls += dppf<ROR8>(ls);
    const float lw = (rdlane(ls, 0) + rdlane(ls, 16)) + (rdlane(ls, 32) + rdlane(ls, 48));
    STAMP(6);
    float oe, oo;
    PVALL(oe, oo, X, L);
    STAMP(7);
    {
      float M;
      int sp = 0;
      while (true) {
        CBAR();
        u32x4 m0 = *(u32x4*)&S.mw[L][0], m1v = *(u32x4*)&S.mw[L][4];
        u32 um = umax3(umax3(m0.x, m0.y, m0.z), umax3(m0.w, m1v.x, m1v.y), umax3(m1v.z, m1v.w, 0u));
        M = fmx(fmx(fmx(u2f(m0.x), u2f(m0.y)), fmx(u2f(m0.z), u2f(m0.w))), fmx(fmx(u2f(m1v.x), u2f(m1v.y)), fmx(u2f(m1v.z), u2f(m1v.w))));
        if (__builtin_amdgcn_readfirstlane(um) != SENT || ++sp > SPIN_L) break;
      }
      const float f = __builtin_amdgcn_exp2f((mwv - M) * LOG2E);
      f32x2 o = {oe * f, oo * f};
      *(f32x2*)&S.part[w][8 * (lane & 15) + 2 * (lane >> 4)] = o;
      if (lane == 0) *(f32x4*)&S.part[w][128] = f32x4{lw * f, w == 0 ? M : 0.f, 0.f, 0.f};
      u32 c = lds_inc(&S.c_part);
      if (c == 8 * L + 7) {
        if (lane < 33) {
          const f32x4* pr = (const f32x4*)&S.part[0][4 * lane];
          f32x4 t = ((pr[0] + pr[33]) + (pr[66] + pr[99])) + ((pr[132] + pr[165]) + (pr[198] + pr[231]));
          st128s(u32x4{clampS(f2u(t.x)), clampS(f2u(t.y)), clampS(f2u(t.z)), clampS(f2u(t.w))}, R, lane * 16, lb + O_REC + (h * 8 + s) * 640);
        }
        STAMP(8);
      }
    }
    if (L == 0) {
      if (role == 0) {
        u32 vo = (64 * w + 16 * (lane >> 4) + 4 * (lane & 3)) * 1280 + ((lane >> 2) & 3) * 16;
        const char* base = Wo + (size_t)(512 * g) * 1280 + j * 64;
        RWLOAD_W(vo, base);
      } else if (role == 1) {
        if (w >= 4) {
          const int sg = 16 * (w - 4) + (lane >> 2);
          u32 vo = 8 * sg * 3456 + (lane & 3) * 16, vo2 = (512 + 2 * sg) * 3456 + (lane & 3) * 16;
          const char* base = W1 + m1 * 64;
          RWLOAD_M1A(vo, base);
          RWLOAD_M1B(vo2, base);
          bias = *(const u32*)(B1 + m1 * 64 + (lane & 15) * 4);
          RWPERM_T();
        }
      } else if (role == 2) {
        const int rg = (lane >> 1) < 27 ? (lane >> 1) : 26;
        u32 vo = (216 * w + 8 * rg) * 1280 + (lane & 1) * 16;
        const char* base = W2 + m2 * 32;
        RWLOAD_M2(vo, base);
        bias = *(const u32*)(B2 + m2 * 32 + (lane & 7) * 4);
      }
      if (w == 1) {
        const u32 tag = S.tag;
        int sp = 0;
        while (true) {
          CBAR();
          u32x4 f = ld128s(R, lane * 16, WS_FLAGS);
          bool ok = f.x == tag && f.y == tag && f.z == tag && f.w == tag;
          if (__builtin_amdgcn_ballot_w64(!ok) == 0 || ++sp > SPIN_G) break;
        }
        CBAR(); S.f_ep = 1; CBAR();
        if (b == 0 && lane == 0) st32s(tag, R, 0, WS_E);
      }
      RWPERM4();
      lds_wait(&S.f_ep, 1);
    }
    STAMP(9);
    float a0, a1, a2, a3, a4, a5, a6, a7;
    if (role == 0) {
      const int c = lane >> 4, sq = (lane >> 2) & 3, e = lane & 3;
      const u32 rb0 = lb + O_REC + (4 * g + (w >> 1)) * 8 * 640;
      const u32 va = sq * 640 + (w & 1) * 256 + (4 * c + e) * 16, vm = (sq + 4 * (e & 1)) * 640 + 512;
      u32x4 A, Bv; u32x2 ML;
      int sp = 0;
      while (true) {
        CBAR();
        A = ld128s(R, va, rb0);
        Bv = ld128s(R, va + 2560, rb0);
        ML = ld64s(R, vm, rb0);
        u32 mxu = umax3(umax3(A.x, A.y, A.z), umax3(A.w, Bv.x, Bv.y), umax3(umax3(Bv.z, Bv.w, ML.x), ML.y, 0u));
        if (__builtin_amdgcn_ballot_w64(mxu == SENT) == 0 || ++sp > SPIN_G) break;
      }
      STAMP(10);
      const float Mr = u2f(ML.y);
      float Mx = fmx(Mr, dppf<QP_X1>(Mr));
      Mx = fmx(Mx, dppf<ROR4>(Mx));
      Mx = fmx(Mx, dppf<ROR8>(Mx));
      const float f = __builtin_amdgcn_exp2f((Mr - Mx) * LOG2E);
      float Ls = u2f(ML.x) * f;
      Ls += dppf<QP_X1>(Ls);
      Ls += dppf<ROR4>(Ls);
      Ls += dppf<ROR8>(Ls);
      const float ri = __builtin_amdgcn_rcpf(Ls);
      const float fa = dppf<QP_B0>(f), fb = dppf<QP_B1>(f);
      float o0 = u2f(A.x) * fa + u2f(Bv.x) * fb, o1 = u2f(A.y) * fa + u2f(Bv.y) * fb;
      float o2 = u2f(A.z) * fa + u2f(Bv.z) * fb, o3 = u2f(A.w) * fa + u2f(Bv.w) * fb;
      o0 += dppf<ROR4>(o0); o1 += dppf<ROR4>(o1); o2 += dppf<ROR4>(o2); o3 += dppf<ROR4>(o3);
      o0 += dppf<ROR8>(o0); o1 += dppf<ROR8>(o1); o2 += dppf<ROR8>(o2); o3 += dppf<ROR8>(o3);
      const u32 p0 = pk(o0 * ri, o1 * ri), p1 = pk(o2 * ri, o3 * ri);
      RGEMV2(p0, p1);
      float t0 = red16(red32(a0, a4), red32(a2, a6)), t1 = red16(red32(a1, a5), red32(a3, a7));
      t0 += dppf<QP_X1>(t0); t1 += dppf<QP_X1>(t1);
      t0 += dppf<QP_X2>(t0); t1 += dppf<QP_X2>(t1);
      if (e == 0) *(f32x2*)&S.wpart[w][8 * sq + 2 * c] = f32x2{t0, t1};
      u32 cc = lds_inc(&S.c_role);
      if (cc == 8 * L + 7) {
        if (lane < 32) {
          const float* r = &S.wpart[0][lane];
          float t = ((r[0] + r[32]) + (r[64] + r[96])) + ((r[128] + r[160]) + (r[192] + r[224]));
          st32s(clampS(f2u(t)), R, lane * 4, lb + O_Z + g * 2560 + j * 128);
        }
        STAMP(11);
      }
    } else if (role == 1) {
      if (w < 5) {
        const int hb = lane >> 5, i = lane & 31;
        const u32 xp = S.xs[64 * w + 2 * i + hb];
        const u32 va = hb * 2560 + w * 512 + i * 16;
        u32x4 z0, z1, z2, z3;
        int sp = 0;
        while (true) {
          CBAR();
          z0 = ld128s(R, va, lb + O_Z); z1 = ld128s(R, va, lb + O_Z + 5120);
          z2 = ld128s(R, va, lb + O_Z + 10240); z3 = ld128s(R, va, lb + O_Z + 15360);
          u32 mxu = umax3(umax3(umax3(z0.x, z0.y, z0.z), umax3(z0.w, z1.x, z1.y), umax3(z1.z, z1.w, z2.x)),
                          umax3(umax3(z2.y, z2.z, z2.w), umax3(z3.x, z3.y, z3.z), z3.w), 0u);
          if (__builtin_amdgcn_ballot_w64(mxu == SENT) == 0 || ++sp > SPIN_G) break;
        }
        STAMP(10);
        f32x4 zs = (__builtin_bit_cast(f32x4, z0) + __builtin_bit_cast(f32x4, z1)) + (__builtin_bit_cast(f32x4, z2) + __builtin_bit_cast(f32x4, z3));
        float za = red32(zs.x, zs.z), zb = red32(zs.y, zs.w);
        S.x1s[64 * w + 2 * i + hb] = pk(lo2f(xp) + rb(za), hi2f(xp) + rb(zb));
        lds_inc_nr(&S.c_x1);
      }
      if (w >= 4) {
        const int sg = 16 * (((lane & 3) + w) & 3) + (lane >> 2);
        lds_wait(&S.c_x1, 5 * (L + 1));
        u32x4 xv = *(const u32x4*)&S.x1s[4 * sg];
        u32 p0 = xv.x, p1 = xv.y, p2 = xv.z, p3 = xv.w, p4 = S.x1s[256 + sg];
#pragma unroll 1
        for (int it = 0; it < 2; it++) LN_PASS(p0, p1, p2, p3, p4);
        p0 = dppu<QP_B0>(p0); p1 = dppu<QP_B0>(p1); p2 = dppu<QP_B0>(p2); p3 = dppu<QP_B0>(p3); p4 = dppu<QP_B0>(p4);
        RGEMV5(p0, p1, p2, p3, p4);
        float t0 = red16(red32(a0, a4), red32(a2, a6)), t1 = red16(red32(a1, a5), red32(a3, a7));
        t0 += dppf<ROR4>(t0); t1 += dppf<ROR4>(t1);
        t0 += dppf<ROR8>(t0); t1 += dppf<ROR8>(t1);
        if ((lane & 12) == 0) *(f32x2*)&S.wpart[w - 4][8 * (lane & 3) + 2 * (lane >> 4)] = f32x2{t0, t1};
        u32 cc = lds_inc(&S.c_role);
        if (cc == 4 * L + 3) {
          if (lane < 16) {
            const f32x2* r = (const f32x2*)&S.wpart[0][2 * lane];
            f32x2 t = (r[0] + r[16]) + (r[32] + r[48]);
            float g0 = rb(rb(t.x) + lo2f(bias)), g1 = rb(rb(t.y) + hi2f(bias));
            const float C1 = -2.f * 0.7978845608028654f * LOG2E, C3 = C1 * 0.044715f;
            float e0 = __builtin_amdgcn_exp2f(g0 * __builtin_fmaf(C3 * g0, g0, C1));
            float e1 = __builtin_amdgcn_exp2f(g1 * __builtin_fmaf(C3 * g1, g1, C1));
            float h0 = g0 * __builtin_amdgcn_rcpf(1.f + e0), h1 = g1 * __builtin_amdgcn_rcpf(1.f + e1);
            st32s(clampS(pk(h0, h1)), R, lane * 4, lb + O_H + m1 * 128);
          }
          STAMP(11);
        }
      }
    } else if (role == 2) {
      if (w == 0) {
        const u32 xp = S.xs[8 * m2 + (lane & 7)];
        u32x2 z;
        int sp = 0;
        while (true) {
          CBAR();
          z = ld64s(R, (lane >> 3) * 2560 + (lane & 7) * 8, lb + O_Z + m2 * 64);
          if (__builtin_amdgcn_ballot_w64(umax3(z.x, z.y, 0u) == SENT) == 0 || ++sp > SPIN_G) break;
        }
        float za = u2f(z.x), zb = u2f(z.y);
        za = red16(za, za); zb = red16(zb, zb);
        za = red32(za, za); zb = red32(zb, zb);
        za += dppf<ROR8>(za); zb += dppf<ROR8>(zb);
        if (lane < 8) S.x1m[lane] = pk(lo2f(xp) + rb(za), hi2f(xp) + rb(zb));
      }
      const int rg = (lane >> 1) < 27 ? (lane >> 1) : 26;
      const int kk = 27 * w + rg;
      const u32 va = (kk >> 2) * 128 + (kk & 3) * 16;
      u32x4 hv;
      int sp = 0;
      while (true) {
        CBAR();
        hv = ld128s(R, va, lb + O_H);
        if (__builtin_amdgcn_ballot_w64(umax3(umax3(hv.x, hv.y, hv.z), hv.w, 0u) == SENT) == 0 || ++sp > SPIN_G) break;
      }
      STAMP(10);
      if (lane >= 54) hv = u32x4{0, 0, 0, 0};
      const u32 p0 = hv.x, p1 = hv.y, p2 = hv.z, p3 = hv.w;
      RGEMV4(p0, p1, p2, p3);
      float t0 = red16(red32(a0, a4), red32(a2, a6)), t1 = red16(red32(a1, a5), red32(a3, a7));
      t0 += dppf<QP_X2>(t0); t1 += dppf<QP_X2>(t1);
      t0 += dppf<ROR4>(t0); t1 += dppf<ROR4>(t1);
      t0 += dppf<ROR8>(t0); t1 += dppf<ROR8>(t1);
      if ((lane & 14) == 0) *(f32x2*)&S.wpart[w][8 * (lane & 1) + 2 * (lane >> 4)] = f32x2{t0, t1};
      u32 cc = lds_inc(&S.c_role);
      if (cc == 8 * L + 7) {
        const int d = lane & 7;
        const f32x2* r = (const f32x2*)&S.wpart[0][2 * d];
        const u32 xr = S.x1m[d];
        f32x2 t = ((r[0] + r[16]) + (r[32] + r[48])) + ((r[64] + r[80]) + (r[96] + r[112]));
        const u32 o = pk(lo2f(xr) + rb(rb(t.x) + lo2f(bias)), hi2f(xr) + rb(rb(t.y) + hi2f(bias)));
        if (L == 0) st32s(clampS(o), R, (lane >> 3) * 8192 + d * 4, lb + O_X + m2 * 128);
        else if (lane < 8) *(u32*)(out + m2 * 32 + d * 4) = o;
        STAMP(11);
      }
    }
  }
}

extern "C" void kernel_launch(void* const* d_in, const int* in_sizes, int n_in, void* d_out, int out_size,
                              void* d_ws, size_t ws_size, hipStream_t stream) {
  fused_kernel<<<256, 512, 0, stream>>>((const char*)d_in[0], (const char*)d_in[1], (const char*)d_in[2],
                                        (const char*)d_in[3], (const char*)d_in[4], (const char*)d_in[6],
                                        (const char*)d_in[7], (const char*)d_in[8], (const char*)d_in[9],
                                        (char*)d_out, (char*)d_ws);
}
```

```cpp
#include <hip/hip_runtime.h>
#include <stdint.h>

__global__ void __launch_bounds__(768, 3)
fused_kernel(const void* xin, const void* Kc, const void* Vc, const void* Wq, const void* Wo, const void* W1,
             const void* B1, const void* W2, const void* B2, void* out, void* ws) {
  __shared__ __attribute__((aligned(16))) char lds[76032];
  register unsigned tid asm("v0") = threadIdx.x;
  asm volatile(
    "s_load_dwordx16 s[8:23], %1, 0\n"
    "s_load_dwordx4 s[24:27], %1, 64\n"
    "s_load_dwordx2 s[28:29], %1, 80\n"
    "v_lshrrev_b32 v1, 6, %0\n"
    "v_and_b32 v0, 63, %0\n"
    "v_readfirstlane_b32 s34, v1\n"
    "s_mov_b32 s30, %2\n"
    "s_and_b32 s31, s30, 7\n"
    "s_lshr_b32 s32, s30, 3\n"
    "s_lshl_b32 s33, s31, 5\n"
    "s_add_u32 s33, s33, s32\n"
    "s_mov_b32 s37, 0\n"
    "s_memrealtime s[96:97]\n"
    "s_cmp_lt_u32 s34, 4\n"
    "s_cbranch_scc1 .Lk_HELPER\n"
    "s_sub_u32 s34, s34, 4\n"
    "s_lshl_b32 s46, s33, 3\n"
    "s_add_u32 s46, s46, s34\n"
    "s_lshl_b32 s46, s46, 14\n"
    "v_and_b32 v1, 12, v0\n"
    "v_and_b32 v2, 3, v0\n"
    "v_lshlrev_b32 v2, 4, v2\n"
    "v_lshl_or_b32 v2, v1, 10, v2\n"
    "v_and_b32 v1, 48, v0\n"
    "v_lshl_or_b32 v2, v1, 2, v2\n"
    "v_lshlrev_b32 v3, 4, v0\n"
    "v_add_u32 v3, 0x1000, v3\n"
    "v_add_u32 v4, 0x2000, v3\n"
    "s_mov_b32 s56, 0x55555555\n"
    "s_mov_b32 s57, 0x55555555\n"
    "s_mov_b32 s58, 0x33333333\n"
    "s_mov_b32 s59, 0x33333333\n"
    "s_waitcnt lgkmcnt(0)\n"
    "s_add_u32 s10, s10, s46\n"
    "s_addc_u32 s11, s11, 0\n"
    "s_add_u32 s12, s12, s46\n"
    "s_addc_u32 s13, s13, 0\n"
    "s_lshr_b32 s46, s46, 14\n"
    "s_mul_i32 s46, s46, 0x9e3779b1\n"
    "s_lshr_b32 s46, s46, 26\n"
    "s_and_b32 s46, s46, 63\n"
    "s_lshl_b32 s46, s46, 8\n"
    "v_xor_b32 v2, s46, v2\n"
    "v_lshlrev_b32 v3, 4, v0\n"
    "v_xor_b32 v3, s46, v3\n"
    "v_xor_b32 v4, 0x0, v2\n"
    "global_load_dwordx4 v[40:43], v4, s[10:11] nt\n"
    "v_xor_b32 v4, 0x800, v2\n"
    "global_load_dwordx4 v[44:47], v4, s[10:11] nt\n"
    "s_barrier\n"
    "v_xor_b32 v4, 0x100, v2\n"
    "global_load_dwordx4 v[48:51], v4, s[10:11] nt\n"
    "v_xor_b32 v4, 0x900, v2\n"
    "global_load_dwordx4 v[52:55], v4, s[10:11] nt\n"
    "v_xor_b32 v4, 0x200, v2\n"
    "global_load_dwordx4 v[56:59], v4, s[10:11] nt\n"
    "s_waitcnt vmcnt(4)\n"
    "v_xor_b32 v4, 0xa00, v2\n"
    "global_load_dwordx4 v[60:63], v4, s[10:11] nt\n"
    "s_waitcnt vmcnt(4)\n"
    "v_xor_b32 v4, 0x300, v2\n"
    "global_load_dwordx4 v[64:67], v4, s[10:11] nt\n"
    "s_waitcnt vmcnt(4)\n"
    "v_xor_b32 v4, 0xb00, v2\n"
    "global_load_dwordx4 v[68:71], v4, s[10:11] nt\n"
    "s_waitcnt vmcnt(4)\n"
    "v_xor_b32 v4, 0x400, v2\n"
    "global_load_dwordx4 v[72:75], v4, s[10:11] nt\n"
    "s_mov_b64 vcc, s[56:57]\n"
    "v_cndmask_b32_dpp v16, v44, v40, vcc quad_perm:[1,0,3,2] row_mask:0xf bank_mask:0xf\n"
    "v_cndmask_b32_dpp v17, v45, v41, vcc quad_perm:[1,0,3,2] row_mask:0xf bank_mask:0xf\n"
    "v_cndmask_b32_dpp v18, v46, v42, vcc quad_perm:[1,0,3,2] row_mask:0xf bank_mask:0xf\n"
    "v_cndmask_b32_dpp v19, v47, v43, vcc quad_perm:[1,0,3,2] row_mask:0xf bank_mask:0xf\n"
    "v_cndmask_b32_dpp v24, v52, v48, vcc quad_perm:[1,0,3,2] row_mask:0xf bank_mask:0xf\n"
    "v_cndmask_b32_dpp v25, v53, v49, vcc quad_perm:[1,0,3,2] row_mask:0xf bank_mask:0xf\n"
    "v_cndmask_b32_dpp v26, v54, v50, vcc quad_perm:[1,0,3,2] row_mask:0xf bank_mask:0xf\n"
    "v_cndmask_b32_dpp v27, v55, v51, vcc quad_perm:[1,0,3,2] row_mask:0xf bank_mask:0xf\n"
    "s_not_b64 vcc, s[56:57]\n"
    "v_cndmask_b32_dpp v20, v40, v44, vcc quad_perm:[1,0,3,2] row_mask:0xf bank_mask:0xf\n"
    "v_cndmask_b32_dpp v21, v41, v45, vcc quad_perm:[1,0,3,2] row_mask:0xf bank_mask:0xf\n"
    "v_cndmask_b32_dpp v22, v42, v46, vcc quad_perm:[1,0,3,2] row_mask:0xf bank_mask:0xf\n"
    "v_cndmask_b32_dpp v23, v43, v47, vcc quad_perm:[1,0,3,2] row_mask:0xf bank_mask:0xf\n"
    "v_cndmask_b32_dpp v28, v48, v52, vcc quad_perm:[1,0,3,2] row_mask:0xf bank_mask:0xf\n"
    "v_cndmask_b32_dpp v29, v49, v53, vcc quad_perm:[1,0,3,2] row_mask:0xf bank_mask:0xf\n"
    "v_cndmask_b32_dpp v30, v50, v54, vcc quad_perm:[1,0,3,2] row_mask:0xf bank_mask:0xf\n"
    "v_cndmask_b32_dpp v31, v51, v55, vcc quad_perm:[1,0,3,2] row_mask:0xf bank_mask:0xf\n"
    "s_mov_b64 vcc, s[58:59]\n"
    "v_cndmask_b32_dpp v40, v24, v16, vcc quad_perm:[2,3,0,1] row_mask:0xf bank_mask:0xf\n"
    "v_cndmask_b32_dpp v41, v25, v17, vcc quad_perm:[2,3,0,1] row_mask:0xf bank_mask:0xf\n"
    "v_cndmask_b32_dpp v42, v26, v18, vcc quad_perm:[2,3,0,1] row_mask:0xf bank_mask:0xf\n"
    "v_cndmask_b32_dpp v43, v27, v19, vcc quad_perm:[2,3,0,1] row_mask:0xf bank_mask:0xf\n"
    "v_cndmask_b32_dpp v44, v28, v20, vcc quad_perm:[2,3,0,1] row_mask:0xf bank_mask:0xf\n"
    "v_cndmask_b32_dpp v45, v29, v21, vcc quad_perm:[2,3,0,1] row_mask:0xf bank_mask:0xf\n"
    "v_cndmask_b32_dpp v46, v30, v22, vcc quad_perm:[2,3,0,1] row_mask:0xf bank_mask:0xf\n"
    "v_cndmask_b32_dpp v47, v31, v23, vcc quad_perm:[2,3,0,1] row_mask:0xf bank_mask:0xf\n"
    "s_not_b64 vcc, s[58:59]\n"
    "v_cndmask_b32_dpp v48, v16, v24, vcc quad_perm:[2,3,0,1] row_mask:0xf bank_mask:0xf\n"
    "v_cndmask_b32_dpp v49, v17, v25, vcc quad_perm:[2,3,0,1] row_mask:0xf bank_mask:0xf\n"
    "v_cndmask_b32_dpp v50, v18, v26, vcc quad_perm:[2,3,0,1] row_mask:0xf bank_mask:0xf\n"
    "v_cndmask_b32_dpp v51, v19, v27, vcc quad_perm:[2,3,0,1] row_mask:0xf bank_mask:0xf\n"
    "v_cndmask_b32_dpp v52, v20, v28, vcc quad_perm:[2,3,0,1] row_mask:0xf bank_mask:0xf\n"
    "v_cndmask_b32_dpp v53, v21, v29, vcc quad_perm:[2,3,0,1] row_mask:0xf bank_mask:0xf\n"
    "v_cndmask_b32_dpp v54, v22, v30, vcc quad_perm:[2,3,0,1] row_mask:0xf bank_mask:0xf\n"
    "v_cndmask_b32_dpp v55, v23, v31, vcc quad_perm:[2,3,0,1] row_mask:0xf bank_mask:0xf\n"
    "s_waitcnt vmcnt(4)\n"
    "v_xor_b32 v4, 0xc00, v2\n"
    "global_load_dwordx4 v[76:79], v4, s[10:11] nt\n"
    "s_waitcnt vmcnt(4)\n"
    "v_xor_b32 v4, 0x500, v2\n"
    "global_load_dwordx4 v[80:83], v4, s[10:11] nt\n"
    "s_waitcnt vmcnt(4)\n"
    "v_xor_b32 v4, 0xd00, v2\n"
    "global_load_dwordx4 v[84:87], v4, s[10:11] nt\n"
    "s_waitcnt vmcnt(4)\n"
    "v_xor_b32 v4, 0x600, v2\n"
    "global_load_dwordx4 v[88:91], v4, s[10:11] nt\n"
    "s_mov_b64 vcc, s[56:57]\n"
    "v_cndmask_b32_dpp v16, v60, v56, vcc quad_perm:[1,0,3,2] row_mask:0xf bank_mask:0xf\n"
    "v_cndmask_b32_dpp v17, v61, v57, vcc quad_perm:[1,0,3,2] row_mask:0xf bank_mask:0xf\n"
    "v_cndmask_b32_dpp v18, v62, v58, vcc quad_perm:[1,0,3,2] row_mask:0xf bank_mask:0xf\n"
    "v_cndmask_b32_dpp v19, v63, v59, vcc quad_perm:[1,0,3,2] row_mask:0xf bank_mask:0xf\n"
    "v_cndmask_b32_dpp v24, v68, v64, vcc quad_perm:[1,0,3,2] row_mask:0xf bank_mask:0xf\n"
    "v_cndmask_b32_dpp v25, v69, v65, vcc quad_perm:[1,0,3,2] row_mask:0xf bank_mask:0xf\n"
    "v_cndmask_b32_dpp v26, v70, v66, vcc quad_perm:[1,0,3,2] row_mask:0xf bank_mask:0xf\n"
    "v_cndmask_b32_dpp v27, v71, v67, vcc quad_perm:[1,0,3,2] row_mask:0xf bank_mask:0xf\n"
    "s_not_b64 vcc, s[56:57]\n"
    "v_cndmask_b32_dpp v20, v56, v60, vcc quad_perm:[1,0,3,2] row_mask:0xf bank_mask:0xf\n"
    "v_cndmask_b32_dpp v21, v57, v61, vcc quad_perm:[1,0,3,2] row_mask:0xf bank_mask:0xf\n"
    "v_cndmask_b32_dpp v22, v58, v62, vcc quad_perm:[1,0,3,2] row_mask:0xf bank_mask:0xf\n"
    "v_cndmask_b32_dpp v23, v59, v63, vcc quad_perm:[1,0,3,2] row_mask:0xf bank_mask:0xf\n"
    "v_cndmask_b32_dpp v28, v64, v68, vcc quad_perm:[1,0,3,2] row_mask:0xf bank_mask:0xf\n"
    "v_cndmask_b32_dpp v29, v65, v69, vcc quad_perm:[1,0,3,2] row_mask:0xf bank_mask:0xf\n"
    "v_cndmask_b32_dpp v30, v66, v70, vcc quad_perm:[1,0,3,2] row_mask:0xf bank_mask:0xf\n"
    "v_cndmask_b32_dpp v31, v67, v71, vcc quad_perm:[1,0,3,2] row_mask:0xf bank_mask:0xf\n"
    "s_mov_b64 vcc, s[58:59]\n"
    "v_cndmask_b32_dpp v56, v24, v16, vcc quad_perm:[2,3,0,1] row_mask:0xf bank_mask:0xf\n"
    "v_cndmask_b32_dpp v57, v25, v17, vcc quad_perm:[2,3,0,1] row_mask:0xf bank_mask:0xf\n"
    "v_cndmask_b32_dpp v58, v26, v18, vcc quad_perm:[2,3,0,1] row_mask:0xf bank_mask:0xf\n"
    "v_cndmask_b32_dpp v59, v27, v19, vcc quad_perm:[2,3,0,1] row_mask:0xf bank_mask:0xf\n"
    "v_cndmask_b32_dpp v60, v28, v20, vcc quad_perm:[2,3,0,1] row_mask:0xf bank_mask:0xf\n"
    "v_cndmask_b32_dpp v61, v29, v21, vcc quad_perm:[2,3,0,1] row_mask:0xf bank_mask:0xf\n"
    "v_cndmask_b32_dpp v62, v30, v22, vcc quad_perm:[2,3,0,1] row_mask:0xf bank_mask:0xf\n"
    "v_cndmask_b32_dpp v63, v31, v23, vcc quad_perm:[2,3,0,1] row_mask:0xf bank_mask:0xf\n"
    "s_not_b64 vcc, s[58:59]\n"
    "v_cndmask_b32_dpp v64, v16, v24, vcc quad_perm:[2,3,0,1] row_mask:0xf bank_mask:0xf\n"
    "v_cndmask_b32_dpp v65, v17, v25, vcc quad_perm:[2,3,0,1] row_mask:0xf bank_mask:0xf\n"
    "v_cndmask_b32_dpp v66, v18, v26, vcc quad_perm:[2,3,0,1] row_mask:0xf bank_mask:0xf\n"
    "v_cndmask_b32_dpp v67, v19, v27, vcc quad_perm:[2,3,0,1] row_mask:0xf bank_mask:0xf\n"
    "v_cndmask_b32_dpp v68, v20, v28, vcc quad_perm:[2,3,0,1] row_mask:0xf bank_mask:0xf\n"
    "v_cndmask_b32_dpp v69, v21, v29, vcc quad_perm:[2,3,0,1] row_mask:0xf bank_mask:0xf\n"
    "v_cndmask_b32_dpp v70, v22, v30, vcc quad_perm:[2,3,0,1] row_mask:0xf bank_mask:0xf\n"
    "v_cndmask_b32_dpp v71, v23, v31, vcc quad_perm:[2,3,0,1] row_mask:0xf bank_mask:0xf\n"
    "s_waitcnt vmcnt(4)\n"
    "v_xor_b32 v4, 0xe00, v2\n"
    "global_load_dwordx4 v[92:95], v4, s[10:11] nt\n"
    "s_waitcnt vmcnt(4)\n"
    "v_xor_b32 v4, 0x700, v2\n"
    "global_load_dwordx4 v[96:99], v4, s[10:11] nt\n"
    "s_waitcnt vmcnt(4)\n"
    "v_xor_b32 v4, 0xf00, v2\n"
    "global_load_dwordx4 v[100:103], v4, s[10:11] nt\n"
    "s_waitcnt vmcnt(4)\n"
    "v_xor_b32 v4, 0x0, v3\n"
    "global_load_dwordx4 v[104:107], v4, s[12:13] nt\n"
    "s_mov_b64 vcc, s[56:57]\n"
    "v_cndmask_b32_dpp v16, v76, v72, vcc quad_perm:[1,0,3,2] row_mask:0xf bank_mask:0xf\n"
    "v_cndmask_b32_dpp v17, v77, v73, vcc quad_perm:[1,0,3,2] row_mask:0xf bank_mask:0xf\n"
    "v_cndmask_b32_dpp v18, v78, v74, vcc quad_perm:[1,0,3,2] row_mask:0xf bank_mask:0xf\n"
    "v_cndmask_b32_dpp v19, v79, v75, vcc quad_perm:[1,0,3,2] row_mask:0xf bank_mask:0xf\n"
    "v_cndmask_b32_dpp v24, v84, v80, vcc quad_perm:[1,0,3,2] row_mask:0xf bank_mask:0xf\n"
    "v_cndmask_b32_dpp v25, v85, v81, vcc quad_perm:[1,0,3,2] row_mask:0xf bank_mask:0xf\n"
    "v_cndmask_b32_dpp v26, v86, v82, vcc quad_perm:[1,0,3,2] row_mask:0xf bank_mask:0xf\n"
    "v_cndmask_b32_dpp v27, v87, v83, vcc quad_perm:[1,0,3,2] row_mask:0xf bank_mask:0xf\n"
    "s_not_b64 vcc, s[56:57]\n"
    "v_cndmask_b32_dpp v20, v72, v76, vcc quad_perm:[1,0,3,2] row_mask:0xf bank_mask:0xf\n"
    "v_cndmask_b32_dpp v21, v73, v77, vcc quad_perm:[1,0,3,2] row_mask:0xf bank_mask:0xf\n"
    "v_cndmask_b32_dpp v22, v74, v78, vcc quad_perm:[1,0,3,2] row_mask:0xf bank_mask:0xf\n"
    "v_cndmask_b32_dpp v23, v75, v79, vcc quad_perm:[1,0,3,2] row_mask:0xf bank_mask:0xf\n"
    "v_cndmask_b32_dpp v28, v80, v84, vcc quad_perm:[1,0,3,2] row_mask:0xf bank_mask:0xf\n"
    "v_cndmask_b32_dpp v29, v81, v85, vcc quad_perm:[1,0,3,2] row_mask:0xf bank_mask:0xf\n"
    "v_cndmask_b32_dpp v30, v82, v86, vcc quad_perm:[1,0,3,2] row_mask:0xf bank_mask:0xf\n"
    "v_cndmask_b32_dpp v31, v83, v87, vcc quad_perm:[1,0,3,2] row_mask:0xf bank_mask:0xf\n"
    "s_mov_b64 vcc, s[58:59]\n"
    "v_cndmask_b32_dpp v72, v24, v16, vcc quad_perm:[2,3,0,1] row_mask:0xf bank_mask:0xf\n"
    "v_cndmask_b32_dpp v73, v25, v17, vcc quad_perm:[2,3,0,1] row_mask:0xf bank_mask:0xf\n"
    "v_cndmask_b32_dpp v74, v26, v18, vcc quad_perm:[2,3,0,1] row_mask:0xf bank_mask:0xf\n"
    "v_cndmask_b32_dpp v75, v27, v19, vcc quad_perm:[2,3,0,1] row_mask:0xf bank_mask:0xf\n"
    "v_cndmask_b32_dpp v76, v28, v20, vcc quad_perm:[2,3,0,1] row_mask:0xf bank_mask:0xf\n"
    "v_cndmask_b32_dpp v77, v29, v21, vcc quad_perm:[2,3,0,1] row_mask:0xf bank_mask:0xf\n"
    "v_cndmask_b32_dpp v78, v30, v22, vcc quad_perm:[2,3,0,1] row_mask:0xf bank_mask:0xf\n"
    "v_cndmask_b32_dpp v79, v31, v23, vcc quad_perm:[2,3,0,1] row_mask:0xf bank_mask:0xf\n"
    "s_not_b64 vcc, s[58:59]\n"
    "v_cndmask_b32_dpp v80, v16, v24, vcc quad_perm:[2,3,0,1] row_mask:0xf bank_mask:0xf\n"
    "v_cndmask_b32_dpp v81, v17, v25, vcc quad_perm:[2,3,0,1] row_mask:0xf bank_mask:0xf\n"
    "v_cndmask_b32_dpp v82, v18, v26, vcc quad_perm:[2,3,0,1] row_mask:0xf bank_mask:0xf\n"
    "v_cndmask_b32_dpp v83, v19, v27, vcc quad_perm:[2,3,0,1] row_mask:0xf bank_mask:0xf\n"
    "v_cndmask_b32_dpp v84, v20, v28, vcc quad_perm:[2,3,0,1] row_mask:0xf bank_mask:0xf\n"
    "v_cndmask_b32_dpp v85, v21, v29, vcc quad_perm:[2,3,0,1] row_mask:0xf bank_mask:0xf\n"
    "v_cndmask_b32_dpp v86, v22, v30, vcc quad_perm:[2,3,0,1] row_mask:0xf bank_mask:0xf\n"
    "v_cndmask_b32_dpp v87, v23, v31, vcc quad_perm:[2,3,0,1] row_mask:0xf bank_mask:0xf\n"
    "s_waitcnt vmcnt(4)\n"
    "v_xor_b32 v4, 0x400, v3\n"
    "global_load_dwordx4 v[108:111], v4, s[12:13] nt\n"
    "s_waitcnt vmcnt(4)\n"
    "v_xor_b32 v4, 0x800, v3\n"
    "global_load_dwordx4 v[112:115], v4, s[12:13] nt\n"
    "s_waitcnt vmcnt(4)\n"
    "v_xor_b32 v4, 0xc00, v3\n"
    "global_load_dwordx4 v[116:119], v4, s[12:13] nt\n"
    "s_waitcnt vmcnt(4)\n"
    "v_xor_b32 v4, 0x1000, v3\n"
    "global_load_dwordx4 v[120:123], v4, s[12:13] nt\n"
    "s_mov_b64 vcc, s[56:57]\n"
    "v_cndmask_b32_dpp v16, v92, v88, vcc quad_perm:[1,0,3,2] row_mask:0xf bank_mask:0xf\n"
    "v_cndmask_b32_dpp v17, v93, v89, vcc quad_perm:[1,0,3,2] row_mask:0xf bank_mask:0xf\n"
    "v_cndmask_b32_dpp v18, v94, v90, vcc quad_perm:[1,0,3,2] row_mask:0xf bank_mask:0xf\n"
    "v_cndmask_b32_dpp v19, v95, v91, vcc quad_perm:[1,0,3,2] row_mask:0xf bank_mask:0xf\n"
    "v_cndmask_b32_dpp v24, v100, v96, vcc quad_perm:[1,0,3,2] row_mask:0xf bank_mask:0xf\n"
    "v_cndmask_b32_dpp v25, v101, v97, vcc quad_perm:[1,0,3,2] row_mask:0xf bank_mask:0xf\n"
    "v_cndmask_b32_dpp v26, v102, v98, vcc quad_perm:[1,0,3,2] row_mask:0xf bank_mask:0xf\n"
    "v_cndmask_b32_dpp v27, v103, v99, vcc quad_perm:[1,0,3,2] row_mask:0xf bank_mask:0xf\n"
    "s_not_b64 vcc, s[56:57]\n"
    "v_cndmask_b32_dpp v20, v88, v92, vcc quad_perm:[1,0,3,2] row_mask:0xf bank_mask:0xf\n"
    "v_cndmask_b32_dpp v21, v89, v93, vcc quad_perm:[1,0,3,2] row_mask:0xf bank_mask:0xf\n"
    "v_cndmask_b32_dpp v22, v90, v94, vcc quad_perm:[1,0,3,2] row_mask:0xf bank_mask:0xf\n"
    "v_cndmask_b32_dpp v23, v91, v95, vcc quad_perm:[1,0,3,2] row_mask:0xf bank_mask:0xf\n"
    "v_cndmask_b32_dpp v28, v96, v100, vcc quad_perm:[1,0,3,2] row_mask:0xf bank_mask:0xf\n"
    "v_cndmask_b32_dpp v29, v97, v101, vcc quad_perm:[1,0,3,2] row_mask:0xf bank_mask:0xf\n"
    "v_cndmask_b32_dpp v30, v98, v102, vcc quad_perm:[1,0,3,2] row_mask:0xf bank_mask:0xf\n"
    "v_cndmask_b32_dpp v31, v99, v103, vcc quad_perm:[1,0,3,2] row_mask:0xf bank_mask:0xf\n"
    "s_mov_b64 vcc, s[58:59]\n"
    "v_cndmask_b32_dpp v88, v24, v16, vcc quad_perm:[2,3,0,1] row_mask:0xf bank_mask:0xf\n"
    "v_cndmask_b32_dpp v89, v25, v17, vcc quad_perm:[2,3,0,1] row_mask:0xf bank_mask:0xf\n"
    "v_cndmask_b32_dpp v90, v26, v18, vcc quad_perm:[2,3,0,1] row_mask:0xf bank_mask:0xf\n"
    "v_cndmask_b32_dpp v91, v27, v19, vcc quad_perm:[2,3,0,1] row_mask:0xf bank_mask:0xf\n"
    "v_cndmask_b32_dpp v92, v28, v20, vcc quad_perm:[2,3,0,1] row_mask:0xf bank_mask:0xf\n"
    "v_cndmask_b32_dpp v93, v29, v21, vcc quad_perm:[2,3,0,1] row_mask:0xf bank_mask:0xf\n"
    "v_cndmask_b32_dpp v94, v30, v22, vcc quad_perm:[2,3,0,1] row_mask:0xf bank_mask:0xf\n"
    "v_cndmask_b32_dpp v95, v31, v23, vcc quad_perm:[2,3,0,1] row_mask:0xf bank_mask:0xf\n"
    "s_not_b64 vcc, s[58:59]\n"
    "v_cndmask_b32_dpp v96, v16, v24, vcc quad_perm:[2,3,0,1] row_mask:0xf bank_mask:0xf\n"
    "v_cndmask_b32_dpp v97, v17, v25, vcc quad_perm:[2,3,0,1] row_mask:0xf bank_mask:0xf\n"
    "v_cndmask_b32_dpp v98, v18, v26, vcc quad_perm:[2,3,0,1] row_mask:0xf bank_mask:0xf\n"
    "v_cndmask_b32_dpp v99, v19, v27, vcc quad_perm:[2,3,0,1] row_mask:0xf bank_mask:0xf\n"
    "v_cndmask_b32_dpp v100, v20, v28, vcc quad_perm:[2,3,0,1] row_mask:0xf bank_mask:0xf\n"
    "v_cndmask_b32_dpp v101, v21, v29, vcc quad_perm:[2,3,0,1] row_mask:0xf bank_mask:0xf\n"
    "v_cndmask_b32_dpp v102, v22, v30, vcc quad_perm:[2,3,0,1] row_mask:0xf bank_mask:0xf\n"
    "v_cndmask_b32_dpp v103, v23, v31, vcc quad_perm:[2,3,0,1] row_mask:0xf bank_mask:0xf\n"
    "s_waitcnt vmcnt(4)\n"
    "v_xor_b32 v4, 0x1400, v3\n"
    "global_load_dwordx4 v[124:127], v4, s[12:13] nt\n"
    "v_xor_b32 v4, 0x1800, v3\n"
    "global_load_dwordx4 v[128:131], v4, s[12:13] nt\n"
    "v_xor_b32 v4, 0x1c00, v3\n"
    "global_load_dwordx4 v[132:135], v4, s[12:13] nt\n"
    "v_xor_b32 v4, 0x2000, v3\n"
    "global_load_dwordx4 v[136:139], v4, s[12:13] nt\n"
    "v_xor_b32 v4, 0x2400, v3\n"
    "global_load_dwordx4 v[140:143], v4, s[12:13] nt\n"
    "v_xor_b32 v4, 0x2800, v3\n"
    "global_load_dwordx4 v[144:147], v4, s[12:13] nt\n"
    "v_xor_b32 v4, 0x2c00, v3\n"
    "global_load_dwordx4 v[148:151], v4, s[12:13] nt\n"
    "v_xor_b32 v4, 0x3000, v3\n"
    "global_load_dwordx4 v[152:155], v4, s[12:13] nt\n"
    "v_xor_b32 v4, 0x3400, v3\n"
    "global_load_dwordx4 v[156:159], v4, s[12:13] nt\n"
    "v_xor_b32 v4, 0x3800, v3\n"
    "global_load_dwordx4 v[160:163], v4, s[12:13] nt\n"
    "v_xor_b32 v4, 0x3c00, v3\n"
    "global_load_dwordx4 v[164:167], v4, s[12:13] nt\n"
    "v_and_b32 v1, 1, v0\n"
    "v_mov_b32 v6, 0x4444\n"
    "s_mov_b32 s46, 0xaaaa\n"
    "v_mad_u32_u24 v6, v1, s46, v6\n"
    "v_lshlrev_b32 v2, 4, v1\n"
    "v_and_b32 v1, 48, v0\n"
    "v_lshl_or_b32 v2, v1, 1, v2\n"
    "v_and_b32 v1, 3, v0\n"
    "v_mul_u32_u24 v7, 0x5555, v1\n"
    "s_lshl_b32 s46, s34, 9\n"
    "v_lshl_add_u32 v3, v0, 3, s46\n"
    "v_lshl_add_u32 v4, v0, 5, s46\n"
    "v_mov_b32 v5, 0\n"
    "v_mov_b32 v8, -1\n"
    "v_mov_b32 v9, -1\n"
    "ds_write_b64 v3, v[8:9] offset:512\n"
    "ds_write_b64 v3, v[8:9] offset:4608\n"
    "s_mov_b32 s54, 0x3db504f3\n"
    "s_mov_b32 s55, 0x3fb8aa3b\n"
    ".Lk_ALOOP:\n"
    "v_mov_b32 v16, 0\n"
    "v_mov_b32 v17, 0\n"
    "v_mov_b32 v18, 0\n"
    "v_mov_b32 v19, 0\n"
    "v_mov_b32 v20, 0\n"
    "v_mov_b32 v21, 0\n"
    "v_mov_b32 v22, 0\n"
    "v_mov_b32 v23, 0\n"
    "v_mov_b32 v24, 0\n"
    "v_mov_b32 v25, 0\n"
    "v_mov_b32 v26, 0\n"
    "v_mov_b32 v27, 0\n"
    "v_mov_b32 v28, 0\n"
    "v_mov_b32 v29, 0\n"
    "v_mov_b32 v30, 0\n"
    "v_mov_b32 v31, 0\n"
    "v_mov_b32 v32, 0\n"
    "v_mov_b32 v33, 0\n"
    "v_mov_b32 v34, 0\n"
    "v_mov_b32 v35, 0\n"
    "v_mov_b32 v36, 0\n"
    "v_mov_b32 v37, 0\n"
    "v_mov_b32 v38, 0\n"
    "v_mov_b32 v39, 0\n"
    "s_cmp_eq_u32 s37, 0\n"
    "s_waitcnt lgkmcnt(0)\n"
    "s_barrier\n"
    "ds_read_b128 v[8:11], v2 offset:0\n"
    "ds_read_b128 v[12:15], v2 offset:128\n"
    "s_waitcnt vmcnt(16) lgkmcnt(0)\n"
    "v_smfmac_f32_16x16x64_bf16 v[16:19], v[8:11], v[40:47], v6\n"
    "v_smfmac_f32_16x16x64_bf16 v[20:23], v[8:11], v[56:63], v6\n"
    "v_smfmac_f32_16x16x64_bf16 v[16:19], v[12:15], v[48:55], v6\n"
    "v_smfmac_f32_16x16x64_bf16 v[20:23], v[12:15], v[64:71], v6\n"
    "v_smfmac_f32_16x16x64_bf16 v[24:27], v[8:11], v[72:79], v6\n"
    "v_smfmac_f32_16x16x64_bf16 v[28:31], v[8:11], v[88:95], v6\n"
    "v_smfmac_f32_16x16x64_bf16 v[24:27], v[12:15], v[80:87], v6\n"
    "v_smfmac_f32_16x16x64_bf16 v[28:31], v[12:15], v[96:103], v6\n"
    "s_nop 2\n"
    "v_add_f32 v16, v16, v17\n"
    "v_add_f32 v20, v20, v21\n"
    "v_max_f32 v1, v16, v20\n"
    "s_nop 0\n"
    "v_mov_b32_dpp v16, v20 quad_perm:[0,1,2,3] row_mask:0x2 bank_mask:0xf\n"
    "v_add_f32 v24, v24, v25\n"
    "v_add_f32 v28, v28, v29\n"
    "v_max3_f32 v1, v1, v24, v28\n"
    "v_mov_b32_dpp v16, v24 quad_perm:[0,1,2,3] row_mask:0x4 bank_mask:0xf\n"
    "s_nop 0\n"
    "v_max_f32_dpp v1, v1, v1 quad_perm:[1,0,3,2] row_mask:0xf bank_mask:0xf\n"
    "v_mov_b32_dpp v16, v28 quad_perm:[0,1,2,3] row_mask:0x8 bank_mask:0xf\n"
    "v_cvt_pk_bf16_f32 v8, 0, v16\n"
    "v_max_f32_dpp v1, v1, v1 quad_perm:[2,3,0,1] row_mask:0xf bank_mask:0xf\n"
    "v_mul_f32 v8, s54, v8\n"
    "v_cvt_pk_bf16_f32 v8, 0, v8\n"
    "v_max_f32_dpp v1, v1, v1 row_ror:4 row_mask:0xf bank_mask:0xf\n"
    "s_nop 1\n"
    "v_max_f32_dpp v1, v1, v1 row_ror:8 row_mask:0xf bank_mask:0xf\n"
    "v_cvt_pk_bf16_f32 v1, 0, v1\n"
    "v_mul_f32 v1, s54, v1\n"
    "v_cvt_pk_bf16_f32 v1, 0, v1\n"
    "v_mul_f32 v9, s55, v1\n"
    "v_fma_f32 v8, v8, s55, -v9\n"
    "v_exp_f32 v8, v8\n"
    "s_nop 0\n"
    "v_cvt_pk_bf16_f32 v1, v8, 0\n"
    "ds_write_b64 v3, v[8:9] offset:512\n"
    "s_nop 0\n"
    "v_mov_b32_dpp v16, v1 row_newbcast:0 row_mask:0xf bank_mask:0xf\n"
    "v_mov_b32_dpp v17, v1 row_newbcast:1 row_mask:0xf bank_mask:0xf\n"
    "v_mov_b32_dpp v18, v1 row_newbcast:2 row_mask:0xf bank_mask:0xf\n"
    "v_mov_b32_dpp v19, v1 row_newbcast:3 row_mask:0xf bank_mask:0xf\n"
    "v_mov_b32_dpp v20, v1 row_newbcast:4 row_mask:0xf bank_mask:0xf\n"
    "v_mov_b32_dpp v21, v1 row_newbcast:5 row_mask:0xf bank_mask:0xf\n"
    "s_cbranch_scc1 .Lk_FIX0\n"
    ".Lk_BK0:\n"
    "s_nop 0\n"
    "v_smfmac_f32_16x16x64_bf16 v[32:35], v[16:19], v[104:111], v7\n"
    "v_mov_b32_dpp v22, v1 row_newbcast:6 row_mask:0xf bank_mask:0xf\n"
    "v_mov_b32_dpp v23, v1 row_newbcast:7 row_mask:0xf bank_mask:0xf\n"
    "v_smfmac_f32_16x16x64_bf16 v[36:39], v[16:19], v[112:119], v7\n"
    "v_mov_b32_dpp v24, v1 row_newbcast:8 row_mask:0xf bank_mask:0xf\n"
    "v_mov_b32_dpp v25, v1 row_newbcast:9 row_mask:0xf bank_mask:0xf\n"
    "s_cbranch_scc1 .Lk_FIX1\n"
    ".Lk_BK1:\n"
    "s_nop 0\n"
    "v_smfmac_f32_16x16x64_bf16 v[32:35], v[20:23], v[120:127], v7\n"
    "v_mov_b32_dpp v26, v1 row_newbcast:10 row_mask:0xf bank_mask:0xf\n"
    "v_mov_b32_dpp v27, v1 row_newbcast:11 row_mask:0xf bank_mask:0xf\n"
    "v_smfmac_f32_16x16x64_bf16 v[36:39], v[20:23], v[128:135], v7\n"
    "v_mov_b32_dpp v28, v1 row_newbcast:12 row_mask:0xf bank_mask:0xf\n"
    "v_mov_b32_dpp v29, v1 row_newbcast:13 row_mask:0xf bank_mask:0xf\n"
    "s_cbranch_scc1 .Lk_FIX2\n"
    ".Lk_BK2:\n"
    "s_nop 0\n"
    "v_smfmac_f32_16x16x64_bf16 v[32:35], v[24:27], v[136:143], v7\n"
    "v_mov_b32_dpp v30, v1 row_newbcast:14 row_mask:0xf bank_mask:0xf\n"
    "v_mov_b32_dpp v31, v1 row_newbcast:15 row_mask:0xf bank_mask:0xf\n"
    "v_smfmac_f32_16x16x64_bf16 v[36:39], v[24:27], v[144:151], v7\n"
    "s_cbranch_scc1 .Lk_FIX3\n"
    ".Lk_BK3:\n"
    "s_nop 0\n"
    "v_smfmac_f32_16x16x64_bf16 v[32:35], v[28:31], v[152:159], v7\n"
    "v_smfmac_f32_16x16x64_bf16 v[36:39], v[28:31], v[160:167], v7\n"
    "v_add_u32 v3, 0x1000, v3\n"
    "s_mov_b64 exec, 0xffff\n"
    "s_nop 4\n"
    "ds_write_b128 v4, v[32:35] offset:8704\n"
    "ds_write_b128 v4, v[36:39] offset:8720\n"
    "s_mov_b64 exec, -1\n"
    "s_waitcnt lgkmcnt(0)\n"
    "s_barrier\n"
    "s_sub_u32 s46, s30, 160\n"
    "s_cmp_lt_u32 s46, 54\n"
    "s_cbranch_scc1 .Lk_AM12\n"
    "s_cmp_lg_u32 s37, 0\n"
    "s_cbranch_scc1 .Lk_AEND\n"
    "s_mov_b32 s37, 1\n"
    "s_barrier\n"
    "s_branch .Lk_ALOOP\n"
    ".Lk_AEND:\n"
    "s_endpgm\n"
    ".Lk_FIX0:\n"
    "s_waitcnt vmcnt(12)\n"
    "v_swap_b32 v106, v112\n"
    "v_swap_b32 v107, v113\n"
    "v_swap_b32 v110, v116\n"
    "v_swap_b32 v111, v117\n"
    "s_branch .Lk_BK0\n"
    ".Lk_FIX1:\n"
    "s_waitcnt vmcnt(8)\n"
    "v_swap_b32 v122, v128\n"
    "v_swap_b32 v123, v129\n"
    "v_swap_b32 v126, v132\n"
    "v_swap_b32 v127, v133\n"
    "s_branch .Lk_BK1\n"
    ".Lk_FIX2:\n"
    "s_waitcnt vmcnt(4)\n"
    "v_swap_b32 v138, v144\n"
    "v_swap_b32 v139, v145\n"
    "v_swap_b32 v142, v148\n"
    "v_swap_b32 v143, v149\n"
    "s_branch .Lk_BK2\n"
    ".Lk_FIX3:\n"
    "s_waitcnt vmcnt(0)\n"
    "v_swap_b32 v154, v160\n"
    "v_swap_b32 v155, v161\n"
    "v_swap_b32 v158, v164\n"
    "v_swap_b32 v159, v165\n"
    "s_branch .Lk_BK3\n"
    ".Lk_AM12:\n"
    "s_cmp_lg_u32 s37, 0\n"
    "s_cbranch_scc1 .Lk_AM12B\n"
    "v_mov_b32 v8, 0\n"
    "ds_read_b32 v8, v8 offset:13908\n"
    "s_mov_b32 s36, s46\n"
    "s_sub_u32 s46, s32, 20\n"
    "s_lshr_b32 s36, s46, 1\n"
    "s_lshl_b32 s36, s36, 4\n"
    "s_and_b32 s44, s46, 1\n"
    "s_add_u32 s36, s36, s44\n"
    "s_add_u32 s36, s36, s31\n"
    "s_cmp_lt_u32 s46, 6\n"
    "s_cselect_b32 s44, s31, 0\n"
    "s_add_u32 s36, s36, s44\n"
    "s_lshl_b32 s46, s34, 14\n"
    "s_lshl_b32 s47, s36, 5\n"
    "s_add_u32 s46, s46, s47\n"
    "s_add_u32 s46, s46, 0xc0000\n"
    "s_cmp_ge_u32 s34, 5\n"
    "s_cselect_b32 s44, 0x2000, 0\n"
    "s_waitcnt lgkmcnt(0)\n"
    "v_readfirstlane_b32 s47, v8\n"
    "s_and_b32 s47, s47, 0x2000\n"
    "s_xor_b32 s47, s47, s44\n"
    "s_add_u32 s46, s46, s47\n"
    "s_add_u32 s40, s28, s46\n"
    "s_addc_u32 s41, s29, 0\n"
    "s_waitcnt vmcnt(0)\n"
    ".Lk_AM12B:\n"
    "s_lshl_b32 s47, s34, 12\n"
    "s_add_u32 s47, s47, 14592\n"
    "v_lshrrev_b32 v1, 4, v0\n"
    "v_lshl_add_u32 v1, v1, 9, s47\n"
    "v_and_b32 v8, 15, v0\n"
    "v_lshl_add_u32 v1, v8, 3, v1\n"
    "ds_read2_b32 v[8:9], v1 offset0:0 offset1:32\n"
    "ds_read2_b32 v[10:11], v1 offset0:64 offset1:96\n"
    "ds_read2_b32 v[16:17], v1 offset0:1 offset1:33\n"
    "ds_read2_b32 v[18:19], v1 offset0:65 offset1:97\n"
    "v_add_u32 v1, 0x800, v1\n"
    "ds_read2_b32 v[12:13], v1 offset0:0 offset1:32\n"
    "ds_read2_b32 v[14:15], v1 offset0:64 offset1:96\n"
    "ds_read2_b32 v[20:21], v1 offset0:1 offset1:33\n"
    "ds_read2_b32 v[22:23], v1 offset0:65 offset1:97\n"
    "v_mov_b32 v28, 0\n"
    "v_mov_b32 v29, 0\n"
    "v_mov_b32 v30, 0\n"
    "v_mov_b32 v31, 0\n"
    "v_and_b32 v38, 1, v0\n"
    "v_mul_u32_u24 v38, 0x5555, v38\n"
    "v_add_u32 v38, 0x8888, v38\n"
    "v_mov_b32 v32, 0\n"
    "v_mov_b32 v33, 0\n"
    "v_mov_b32 v34, 0\n"
    "v_mov_b32 v35, 0\n"
    "v_bfe_u32 v36, v0, 2, 2\n"
    "v_and_b32 v37, 3, v0\n"
    "v_lshlrev_b32 v37, 3, v37\n"
    "v_lshl_add_u32 v36, v36, 11, v37\n"
    "v_and_b32 v37, 48, v0\n"
    "s_waitcnt lgkmcnt(0)\n"
    "s_barrier\n"
    "s_barrier\n"
    "ds_read_b128 v[24:27], v37 offset:14464\n"
    "s_waitcnt lgkmcnt(0)\n"
    "v_smfmac_f32_16x16x64_bf16 v[28:31], v[24:27], v[8:15], v38\n"
    "v_smfmac_f32_16x16x64_bf16 v[32:35], v[24:27], v[16:23], v38\n"
    "s_mov_b64 exec, 0xffff\n"
    "s_nop 5\n"
    "v_cvt_pk_bf16_f32 v28, v28, v29\n"
    "v_cvt_pk_bf16_f32 v29, v32, v33\n"
    "global_store_dwordx2 v36, v[28:29], s[40:41] sc1\n"
    "s_mov_b64 exec, -1\n"
    "s_cmp_lg_u32 s37, 0\n"
    "s_cbranch_scc1 .Lk_AEND\n"
    "s_mov_b32 s37, 1\n"
    "s_add_u32 s40, s40, 0x100000\n"
    "s_addc_u32 s41, s41, 0\n"
    "s_branch .Lk_ALOOP\n"
    ".Lk_HELPER:\n"
    "s_setprio 3\n"
    "s_and_b32 s47, s33, 3\n"
    "s_lshl_b32 s46, s47, 2\n"
    "s_add_u32 s46, s46, s34\n"
    "s_mul_i32 s46, s46, 327680\n"
    "s_lshr_b32 s44, s33, 2\n"
    "s_lshl_b32 s44, s44, 7\n"
    "s_add_u32 s46, s46, s44\n"
    "v_lshrrev_b32 v4, 3, v0\n"
    "v_and_b32 v5, 7, v0\n"
    "v_lshl_add_u32 v1, v4, 13, s46\n"
    "v_lshl_add_u32 v1, v5, 4, v1\n"
    "v_lshlrev_b32 v34, 4, v0\n"
    "v_lshlrev_b32 v35, 2, v0\n"
    "v_lshrrev_b32 v6, 4, v0\n"
    "v_and_b32 v7, 3, v0\n"
    "v_lshl_add_u32 v6, v6, 2, v7\n"
    "s_mul_i32 s46, s47, 320\n"
    "v_lshl_add_u32 v122, v6, 4, s46\n"
    "v_lshl_add_u32 v123, v6, 2, s46\n"
    "s_lshl_b32 s46, s31, 8\n"
    "s_add_u32 s46, s46, s33\n"
    "s_lshl_b32 s46, s46, 3\n"
    "s_add_u32 s46, s46, 0x74000\n"
    "v_mov_b32 v3, s46\n"
    "s_andn2_b32 s46, s46, 63\n"
    "s_lshr_b32 s44, s34, 1\n"
    "s_lshl_b32 s44, s44, 5\n"
    "s_add_u32 s46, s46, s44\n"
    "v_lshl_add_u32 v8, v7, 3, s46\n"
    "s_lshl_b32 s46, s34, 2\n"
    "v_mov_b32 v165, s46\n"
    "v_mov_b32 v164, 0\n"
    "ds_write_b32 v165, v164 offset:13888\n"
    "s_lshr_b32 s46, s33, 4\n"
    "s_lshl_b32 s46, s46, 14\n"
    "s_and_b32 s44, s33, 15\n"
    "s_lshl_b32 s44, s44, 9\n"
    "s_add_u32 s46, s46, s44\n"
    "s_mul_i32 s44, s34, 0x2080\n"
    "s_andn2_b32 s44, s44, 0x4000\n"
    "s_add_u32 s46, s46, s44\n"
    "v_mov_b32 v166, s46\n"
    "v_xor_b32 v167, 0x2000, v166\n"
    "s_mov_b64 exec, 1\n"
    "s_waitcnt lgkmcnt(0)\n"
    "global_load_dword v166, v166, s[28:29] sc1\n"
    "global_load_dword v167, v167, s[28:29] sc1\n"
    "s_mov_b64 exec, -1\n"
    "global_load_dwordx4 v[40:43], v1, s[14:15] nt\n"
    "v_add_u32 v1, 0x10000, v1\n"
    "global_load_dwordx4 v[44:47], v1, s[14:15] nt\n"
    "v_add_u32 v1, 0x10000, v1\n"
    "global_load_dwordx4 v[48:51], v1, s[14:15] nt\n"
    "v_add_u32 v1, 0x10000, v1\n"
    "global_load_dwordx4 v[52:55], v1, s[14:15] nt\n"
    "v_add_u32 v1, 0x10000, v1\n"
    "global_load_dwordx4 v[56:59], v1, s[14:15] nt\n"
    "global_load_dwordx4 v[16:19], v34, s[8:9]\n"
    "global_load_dword v20, v35, s[8:9] offset:1024\n"
    "global_load_dwordx4 v[152:155], v122, s[8:9]\n"
    "global_load_dword v156, v123, s[8:9] offset:256\n"
    "global_load_dword v39, v3, s[28:29] sc1\n"
    "global_load_dword v37, v8, s[28:29] sc1\n"
    "s_barrier\n"
    "s_waitcnt vmcnt(12)\n"
    "s_memtime s[92:93]\n"
    "s_waitcnt vmcnt(11)\n"
    "s_memtime s[94:95]\n"
    "s_mul_i32 s46, s34, 320\n"
    "v_lshl_add_u32 v2, v4, 3, s46\n"
    "v_mul_u32_u24 v3, 2560, v5\n"
    "v_add_u32 v2, v2, v3\n"
    "s_cmp_lt_u32 s32, 20\n"
    "s_cselect_b32 s35, 0, 2\n"
    "s_mul_i32 s36, s31, 5\n"
    "s_add_u32 s36, s36, s32\n"
    "s_sub_u32 s36, s36, 27\n"
    "s_sub_u32 s46, s30, 160\n"
    "s_cmp_lt_u32 s46, 56\n"
    "s_cselect_b32 s35, 3, s35\n"
    "s_cmp_lt_u32 s46, 54\n"
    "s_cselect_b32 s35, 1, s35\n"
    "s_cselect_b32 s36, s46, s36\n"
    "v_mov_b32 v36, 0\n"
    "s_cbranch_scc0 .Lk_RM1X\n"
    "s_sub_u32 s46, s32, 20\n"
    "s_lshr_b32 s36, s46, 1\n"
    "s_lshl_b32 s36, s36, 4\n"
    "s_and_b32 s44, s46, 1\n"
    "s_add_u32 s36, s36, s44\n"
    "s_add_u32 s36, s36, s31\n"
    "s_cmp_lt_u32 s46, 6\n"
    "s_cselect_b32 s44, s31, 0\n"
    "s_add_u32 s36, s36, s44\n"
    ".Lk_RM1X:\n"
    "v_and_b32 v3, 1, v0\n"
    "v_mov_b32 v160, 0x8888\n"
    "s_movk_i32 s46, 0x5555\n"
    "v_mad_u32_u24 v160, v3, s46, v160\n"
    "v_bfe_u32 v8, v0, 2, 2\n"
    "s_mul_i32 s46, s34, 5120\n"
    "v_mul_u32_u24 v9, 1280, v8\n"
    "v_add_u32 v9, s46, v9\n"
    "v_lshl_add_u32 v1, v6, 6, v9\n"
    "v_lshl_add_u32 v9, v6, 4, v9\n"
    "s_lshr_b32 s46, s33, 4\n"
    "s_lshl_b32 s46, s46, 14\n"
    "s_and_b32 s47, s33, 15\n"
    "s_lshl_b32 s47, s47, 9\n"
    "s_add_u32 s46, s46, s47\n"
    "v_lshl_add_u32 v13, v0, 2, s46\n"
    "s_andn2_b32 s44, s46, 0xe00\n"
    "s_and_b32 s47, s33, 7\n"
    "s_lshl_b32 s45, s47, 6\n"
    "s_add_u32 s45, s45, s44\n"
    "s_lshl_b32 s47, s47, 4\n"
    "s_add_u32 s44, s44, s47\n"
    "s_add_u32 s99, s44, 2048\n"
    "v_and_b32 v13, 15, v0\n"
    "v_lshl_add_u32 v13, v13, 2, s45\n"
    "v_lshrrev_b32 v10, 4, v0\n"
    "v_lshl_add_u32 v13, v10, 9, v13\n"
    "s_cmp_eq_u32 s34, 1\n"
    "s_cbranch_scc0 .Lk_NRP1\n"
    "v_mov_b32 v13, s99\n"
    ".Lk_NRP1:\n"
    "s_add_u32 s46, s46, 0x80000\n"
    "s_lshl_b32 s47, s34, 7\n"
    "s_add_u32 s47, s47, s46\n"
    "v_lshl_add_u32 v10, v8, 5, s47\n"
    "v_lshrrev_b32 v8, 4, v0\n"
    "v_lshl_add_u32 v10, v8, 3, v10\n"
    "v_mov_b32 v8, v3\n"
    "v_lshl_add_u32 v10, v8, 13, v10\n"
    "s_andn2_b32 s46, s46, 0xe00\n"
    "s_lshr_b32 s47, s34, 1\n"
    "s_lshl_b32 s47, s47, 11\n"
    "s_add_u32 s46, s46, s47\n"
    "s_and_b32 s47, s34, 1\n"
    "s_lshl_b32 s44, s47, 8\n"
    "s_add_u32 s46, s46, s44\n"
    "v_lshl_add_u32 v11, v7, 9, s46\n"
    "v_and_b32 v8, 60, v0\n"
    "v_lshl_add_u32 v11, v8, 2, v11\n"
    "s_lshl_b32 s47, s47, 3\n"
    "s_and_b32 s46, s34, 2\n"
    "s_lshl_b32 s46, s46, 4\n"
    "s_add_u32 s46, s46, s47\n"
    "v_and_b32 v8, 52, v0\n"
    "v_lshl_add_u32 v12, v8, 2, s46\n"
    "v_bfe_u32 v8, v0, 3, 1\n"
    "v_lshl_add_u32 v12, v8, 2, v12\n"
    "s_lshl_b32 s38, s34, 2\n"
    "s_cmp_eq_u32 s35, 0\n"
    "s_cbranch_scc0 .Lk_PB1\n"
    "s_mul_i32 s46, s32, 205\n"
    "s_lshr_b32 s46, s46, 10\n"
    "s_mul_i32 s47, s46, 15744\n"
    "s_lshl_b32 s46, s32, 7\n"
    "s_add_u32 s47, s47, s46\n"
    "s_lshr_b32 s46, s31, 1\n"
    "s_mul_i32 s46, s46, 636\n"
    "s_lshl1_add_u32 s46, s31, s46\n"
    "s_add_u32 s47, s47, s46\n"
    "s_add_u32 s47, s47, 0x40000\n"
    "v_and_b32 v4, 31, v0\n"
    "v_lshrrev_b32 v5, 5, v0\n"
    "s_lshr_b32 s38, 0x0, s38\n"
    "s_mov_b32 s98, 0x0\n"
    "s_branch .Lk_PB3\n"
    ".Lk_PB1:\n"
    "s_cmp_eq_u32 s35, 2\n"
    "s_cbranch_scc1 .Lk_PB2\n"
    "s_lshl_b32 s47, s36, 6\n"
    "s_add_u32 s47, s47, 0x50000\n"
    "s_lshr_b32 s38, 0x22, s38\n"
    "s_mov_b32 s98, 0x0\n"
    "s_cmp_eq_u32 s35, 1\n"
    "s_cselect_b32 s47, s47, 0x7c000\n"
    "s_cselect_b32 s38, s38, 7\n"
    "v_and_b32 v4, 15, v0\n"
    "v_bfe_u32 v5, v0, 4, 1\n"
    "s_branch .Lk_PB3\n"
    ".Lk_PB2:\n"
    "s_lshl_b32 s47, s36, 5\n"
    "s_add_u32 s47, s47, 0x54000\n"
    "s_lshr_b32 s38, 0x0, s38\n"
    "s_mov_b32 s98, 0x0\n"
    "v_and_b32 v4, 7, v0\n"
    "v_lshrrev_b32 v5, 3, v0\n"
    ".Lk_PB3:\n"
    "s_and_b32 s38, s38, 15\n"
    "v_lshl_add_u32 v15, v4, 2, s47\n"
    "v_lshl_add_u32 v15, v5, 13, v15\n"
    "s_mov_b64 s[40:41], s[28:29]\n"
    "s_mov_b64 s[42:43], s[28:29]\n"
    "s_add_u32 s52, s28, 0x2000\n"
    "s_addc_u32 s53, s29, 0\n"
    "s_mov_b32 s48, 0x3f803f80\n"
    "s_mov_b32 s49, 0xffff0000\n"
    "s_mov_b32 s54, 0x3acccccd\n"
    "s_mov_b32 s55, 0x3727c5ac\n"
    "s_mov_b32 s56, 0x05040100\n"
    "s_mov_b32 s57, 0x07060302\n"
    "s_mov_b32 s60, 0x33333333\n"
    "s_mov_b32 s61, 0x33333333\n"
    "v_mov_b32 v164, 1\n"
    "v_mov_b32 v3, 0\n"
    "s_waitcnt lgkmcnt(0)\n"
    "s_sub_u32 s46, s94, s92\n"
    "s_min_u32 s46, s46, 511\n"
    "v_lshl_add_u32 v164, s46, 1, v164\n"
    "s_waitcnt vmcnt(0)\n"
    "ds_write_b64 v2, v[40:41] offset:55552\n"
    "ds_write_b64 v2, v[42:43] offset:56832\n"
    "ds_write_b64 v2, v[44:45] offset:55616\n"
    "ds_write_b64 v2, v[46:47] offset:56896\n"
    "ds_write_b64 v2, v[48:49] offset:55680\n"
    "ds_write_b64 v2, v[50:51] offset:56960\n"
    "ds_write_b64 v2, v[52:53] offset:55744\n"
    "ds_write_b64 v2, v[54:55] offset:57024\n"
    "ds_write_b64 v2, v[56:57] offset:55808\n"
    "ds_write_b64 v2, v[58:59] offset:57088\n"
    "v_add_u32 v39, 0x7f4a7c15, v39\n"
    "v_add_u32 v37, 0x7f4a7c15, v37\n"
    "s_cmp_eq_u32 s34, 3\n"
    "s_waitcnt lgkmcnt(0)\n"
    "ds_write_b32 v165, v164 offset:13888\n"
    "s_cbranch_scc0 .Lk_NOCLR\n"
    "v_readfirstlane_b32 s46, v13\n"
    "v_and_b32 v4, 3, v0\n"
    "v_bfe_u32 v5, v0, 2, 2\n"
    "v_lshl_add_u32 v4, v4, 4, s46\n"
    "v_lshl_add_u32 v4, v5, 9, v4\n"
    "v_writelane_b32 v4, s99, 16\n"
    "v_writelane_b32 v4, s99, 48\n"
    "v_and_b32 v6, -4, v15\n"
    "v_lshrrev_b32 v5, 5, v0\n"
    "v_lshl_add_u32 v4, v5, 13, v4\n"
    "v_mov_b32 v24, -1\n"
    "v_mov_b32 v25, -1\n"
    "v_mov_b32 v26, -1\n"
    "v_mov_b32 v27, -1\n"
    "s_add_u32 s44, s28, 0x100000\n"
    "s_addc_u32 s45, s29, 0\n"
    "s_lshl_b32 s46, s33, 3\n"
    "s_add_u32 s46, s46, 0x74000\n"
    "v_and_b32 v5, 7, v0\n"
    "v_lshl_add_u32 v22, v5, 11, s46\n"
    "s_mov_b64 exec, 0xff\n"
    "global_load_dword v21, v22, s[28:29] sc1\n"
    "s_mov_b32 exec_lo, 0x1ffff\n"
    "s_mov_b32 exec_hi, 0x1ffff\n"
    "global_store_dwordx4 v4, v[24:27], s[28:29] sc1\n"
    "global_store_dwordx4 v4, v[24:27], s[44:45] sc1\n"
    "s_mov_b64 exec, -1\n"
    "global_store_dword v6, v24, s[28:29] sc1\n"
    "global_store_dword v6, v24, s[44:45] sc1\n"
    "s_cmp_eq_u32 s35, 2\n"
    "s_cbranch_scc0 .Lk_NOCLP\n"
    "s_lshr_b32 s46, s36, 2\n"
    "s_lshl_b32 s46, s46, 14\n"
    "s_and_b32 s47, s36, 3\n"
    "s_lshl_b32 s47, s47, 11\n"
    "s_add_u32 s46, s46, s47\n"
    "s_add_u32 s46, s46, 0xc0000\n"
    "v_lshl_add_u32 v6, v0, 4, s46\n"
    "v_add_u32 v5, 0x2000, v6\n"
    "global_store_dwordx4 v6, v[24:27], s[28:29] offset:0 sc1\n"
    "global_store_dwordx4 v6, v[24:27], s[28:29] offset:1024 sc1\n"
    "global_store_dwordx4 v5, v[24:27], s[28:29] offset:0 sc1\n"
    "global_store_dwordx4 v5, v[24:27], s[28:29] offset:1024 sc1\n"
    "global_store_dwordx4 v6, v[24:27], s[44:45] offset:0 sc1\n"
    "global_store_dwordx4 v6, v[24:27], s[44:45] offset:1024 sc1\n"
    "global_store_dwordx4 v5, v[24:27], s[44:45] offset:0 sc1\n"
    "global_store_dwordx4 v5, v[24:27], s[44:45] offset:1024 sc1\n"
    ".Lk_NOCLP:\n"
    "s_cmp_eq_u32 s30, 160\n"
    "s_cbranch_scc0 .Lk_NOCLR\n"
    "v_lshlrev_b32 v6, 13, v0\n"
    "v_add_u32 v6, 0x54800, v6\n"
    "s_mov_b64 exec, 0xff\n"
    "global_store_dword v6, v24, s[28:29] sc1\n"
    "s_mov_b64 exec, -1\n"
    ".Lk_NOCLR:\n"
    "s_mov_b32 s39, 0x100000\n"
    ".Lk_P_HF:\n"
    "ds_read_b128 v[4:7], v3 offset:13888\n"
    "s_waitcnt lgkmcnt(0)\n"
    "v_and_b32 v166, v4, v5\n"
    "v_and_b32 v167, v6, v7\n"
    "v_and_b32 v166, v166, v167\n"
    "v_and_b32 v166, 1, v166\n"
    "v_cmp_ne_u32 vcc, 1, v166\n"
    "s_cbranch_vccnz .Lk_R_HF\n"
    ".Lk_D_HF:\n"
    "v_add_u32 v23, v4, v6\n"
    "v_add_u32 v5, v5, v7\n"
    "v_sub_u32 v23, v23, v5\n"
    "ds_read_b128 v[60:63], v1 offset:55552\n"
    "ds_read_b128 v[64:67], v1 offset:55568\n"
    "ds_read_b128 v[68:71], v1 offset:55584\n"
    "ds_read_b128 v[72:75], v1 offset:55600\n"
    "ds_read_b128 v[76:79], v9 offset:56576\n"
    "s_waitcnt lgkmcnt(0)\n"
    "v_perm_b32 v40, v62, v60, s56\n"
    "v_perm_b32 v41, v62, v60, s57\n"
    "v_perm_b32 v42, v63, v61, s56\n"
    "v_perm_b32 v43, v63, v61, s57\n"
    "v_perm_b32 v44, v66, v64, s56\n"
    "v_perm_b32 v45, v66, v64, s57\n"
    "v_perm_b32 v46, v67, v65, s56\n"
    "v_perm_b32 v47, v67, v65, s57\n"
    "v_perm_b32 v48, v70, v68, s56\n"
    "v_perm_b32 v49, v70, v68, s57\n"
    "v_perm_b32 v50, v71, v69, s56\n"
    "v_perm_b32 v51, v71, v69, s57\n"
    "v_perm_b32 v52, v74, v72, s56\n"
    "v_perm_b32 v53, v74, v72, s57\n"
    "v_perm_b32 v54, v75, v73, s56\n"
    "v_perm_b32 v55, v75, v73, s57\n"
    "v_perm_b32 v56, v78, v76, s56\n"
    "v_perm_b32 v57, v78, v76, s57\n"
    "v_perm_b32 v58, v79, v77, s56\n"
    "v_perm_b32 v59, v79, v77, s57\n"
    "s_mov_b32 s56, 0xbdd2d3e8\n"
    "s_mov_b32 s57, 0xc0135761\n"
    "s_mov_b32 s58, 0x3f800054\n"
    "s_branch .Lk_LNQ\n"
    ".Lk_HLOOP:\n"
    "v_mov_b32 v2, 0\n"
    "s_movk_i32 s39, 0x4000\n"
    ".Lk_P_HU:\n"
    "global_load_dword v1, v2, s[50:51] offset:2048 sc1\n"
    "s_waitcnt vmcnt(0)\n"
    "v_cmp_eq_u32 vcc, -1, v1\n"
    "s_cbranch_vccnz .Lk_R_HU\n"
    ".Lk_D_HU:\n"
    "v_readfirstlane_b32 s62, v1\n"
    ".Lk_G_X:\n"
    "s_memrealtime s[44:45]\n"
    "s_waitcnt lgkmcnt(0)\n"
    "s_sub_u32 s44, s44, s62\n"
    "s_cmp_lt_u32 s44, 125\n"
    "s_cbranch_scc1 .Lk_G_X\n"
    "s_movk_i32 s39, 0x4000\n"
    ".Lk_P_X:\n"
    "global_load_dwordx4 v[16:19], v34, s[50:51] sc1\n"
    "global_load_dword v20, v35, s[50:51] offset:1024 sc1\n"
    "global_load_dwordx4 v[152:155], v122, s[50:51] sc1\n"
    "global_load_dword v156, v123, s[50:51] offset:256 sc1\n"
    "s_waitcnt vmcnt(0)\n"
    "v_max3_u32 v1, v16, v17, v18\n"
    "v_max3_u32 v1, v1, v19, v20\n"
    "v_max3_u32 v1, v1, v152, v153\n"
    "v_max3_u32 v1, v1, v154, v155\n"
    "v_max_u32 v1, v1, v156\n"
    "v_cmp_eq_u32 vcc, -1, v1\n"
    "s_cbranch_vccnz .Lk_R_X\n"
    ".Lk_D_X:\n"
    ".Lk_LNQ:\n"
    "v_dot2_f32_bf16 v1, v16, s48, 0\n"
    "v_dot2_f32_bf16 v2, v16, v16, 0\n"
    "v_dot2_f32_bf16 v1, v17, s48, v1\n"
    "v_dot2_f32_bf16 v2, v17, v17, v2\n"
    "v_dot2_f32_bf16 v1, v18, s48, v1\n"
    "v_dot2_f32_bf16 v2, v18, v18, v2\n"
    "v_dot2_f32_bf16 v1, v19, s48, v1\n"
    "v_dot2_f32_bf16 v2, v19, v19, v2\n"
    "v_dot2_f32_bf16 v1, v20, s48, v1\n"
    "v_dot2_f32_bf16 v2, v20, v20, v2\n"
    "v_lshlrev_b32 v24, 16, v152\n"
    "v_and_b32 v25, s49, v152\n"
    "v_lshlrev_b32 v26, 16, v153\n"
    "v_permlane32_swap_b32 v1, v2\n"
    "v_add_f32 v1, v1, v2\n"
    "v_and_b32 v27, s49, v153\n"
    "v_lshlrev_b32 v28, 16, v154\n"
    "v_add_f32_dpp v1, v1, v1 quad_perm:[1,0,3,2] row_mask:0xf bank_mask:0xf\n"
    "v_and_b32 v29, s49, v154\n"
    "v_lshlrev_b32 v30, 16, v155\n"
    "v_add_f32_dpp v1, v1, v1 quad_perm:[2,3,0,1] row_mask:0xf bank_mask:0xf\n"
    "v_and_b32 v31, s49, v155\n"
    "v_lshlrev_b32 v32, 16, v156\n"
    "v_add_f32_dpp v1, v1, v1 row_ror:4 row_mask:0xf bank_mask:0xf\n"
    "v_and_b32 v33, s49, v156\n"
    "s_nop 0\n"
    "v_add_f32_dpp v1, v1, v1 row_ror:8 row_mask:0xf bank_mask:0xf\n"
    "s_nop 1\n"
    "v_add_f32_dpp v1, v1, v1 row_bcast:15 row_mask:0xa bank_mask:0xf\n"
    "v_mul_f32 v1, s54, v1\n"
    "s_nop 0\n"
    "v_readlane_b32 s46, v1, 31\n"
    "v_readlane_b32 s47, v1, 63\n"
    "s_nop 0\n"
    "v_mul_f32 v2, s46, s46\n"
    "v_cvt_pk_bf16_f32 v4, 0, -s46\n"
    "v_sub_f32 v2, s47, v2\n"
    "v_cvt_pk_bf16_f32 v2, 0, |v2|\n"
    "v_add_f32 v2, s55, v2\n"
    "v_cvt_pk_bf16_f32 v2, 0, v2\n"
    "v_sqrt_f32 v2, v2\n"
    "v_pk_add_f32 v[24:25], v[24:25], v[4:5] op_sel_hi:[1,0]\n"
    "v_cvt_pk_bf16_f32 v24, 0, v24\n"
    "v_cvt_pk_bf16_f32 v25, 0, v25\n"
    "v_cvt_pk_bf16_f32 v2, 0, v2\n"
    "v_rcp_f32 v2, v2\n"
    "v_pk_add_f32 v[26:27], v[26:27], v[4:5] op_sel_hi:[1,0]\n"
    "v_cvt_pk_bf16_f32 v26, 0, v26\n"
    "v_cvt_pk_bf16_f32 v27, 0, v27\n"
    "v_pk_add_f32 v[28:29], v[28:29], v[4:5] op_sel_hi:[1,0]\n"
    "v_cvt_pk_bf16_f32 v28, 0, v28\n"
    "v_cvt_pk_bf16_f32 v29, 0, v29\n"
    "v_pk_add_f32 v[30:31], v[30:31], v[4:5] op_sel_hi:[1,0]\n"
    "v_cvt_pk_bf16_f32 v30, 0, v30\n"
    "v_cvt_pk_bf16_f32 v31, 0, v31\n"
    "v_pk_add_f32 v[32:33], v[32:33], v[4:5] op_sel_hi:[1,0]\n"
    "v_cvt_pk_bf16_f32 v32, 0, v32\n"
    "v_cvt_pk_bf16_f32 v33, 0, v33\n"
    "v_pk_mul_f32 v[24:25], v[24:25], v[2:3] op_sel_hi:[1,0]\n"
    "v_cvt_pk_bf16_f32 v24, v24, v25\n"
    "v_pk_mul_f32 v[26:27], v[26:27], v[2:3] op_sel_hi:[1,0]\n"
    "v_cvt_pk_bf16_f32 v26, v26, v27\n"
    "v_pk_mul_f32 v[28:29], v[28:29], v[2:3] op_sel_hi:[1,0]\n"
    "v_cvt_pk_bf16_f32 v28, v28, v29\n"
    "v_pk_mul_f32 v[30:31], v[30:31], v[2:3] op_sel_hi:[1,0]\n"
    "v_cvt_pk_bf16_f32 v30, v30, v31\n"
    "v_pk_mul_f32 v[32:33], v[32:33], v[2:3] op_sel_hi:[1,0]\n"
    "v_cvt_pk_bf16_f32 v32, v32, v33\n"
    "v_dot2_f32_bf16 v6, v40, v24, 0\n"
    "v_dot2_f32_bf16 v7, v41, v24, 0\n"
    "v_dot2_f32_bf16 v8, v42, v24, 0\n"
    "v_dot2_f32_bf16 v9, v43, v24, 0\n"
    "v_dot2_f32_bf16 v6, v44, v26, v6\n"
    "v_dot2_f32_bf16 v7, v45, v26, v7\n"
    "v_dot2_f32_bf16 v8, v46, v26, v8\n"
    "v_dot2_f32_bf16 v9, v47, v26, v9\n"
    "v_dot2_f32_bf16 v6, v48, v28, v6\n"
    "v_dot2_f32_bf16 v7, v49, v28, v7\n"
    "v_dot2_f32_bf16 v8, v50, v28, v8\n"
    "v_dot2_f32_bf16 v9, v51, v28, v9\n"
    "v_dot2_f32_bf16 v6, v52, v30, v6\n"
    "v_dot2_f32_bf16 v7, v53, v30, v7\n"
    "v_dot2_f32_bf16 v8, v54, v30, v8\n"
    "v_dot2_f32_bf16 v9, v55, v30, v9\n"
    "v_dot2_f32_bf16 v6, v56, v32, v6\n"
    "v_dot2_f32_bf16 v7, v57, v32, v7\n"
    "v_dot2_f32_bf16 v8, v58, v32, v8\n"
    "v_dot2_f32_bf16 v9, v59, v32, v9\n"
    "s_nop 1\n"
    "v_permlane32_swap_b32 v6, v8\n"
    "v_permlane32_swap_b32 v7, v9\n"
    "v_add_f32 v1, v6, v8\n"
    "v_add_f32 v2, v7, v9\n"
    "s_cmp_eq_u32 s37, 0\n"
    "s_nop 0\n"
    "v_permlane16_swap_b32 v1, v2\n"
    "v_add_f32 v1, v1, v2\n"
    "s_nop 1\n"
    "v_add_f32_dpp v1, v1, v1 quad_perm:[1,0,3,2] row_mask:0xf bank_mask:0xf\n"
    "s_nop 1\n"
    "v_add_f32_dpp v38, v1, v1 quad_perm:[2,3,0,1] row_mask:0xf bank_mask:0xf\n"
    "s_mov_b64 exec, s[60:61]\n"
    "global_store_dwordx2 v10, v[38:39], s[40:41] sc1\n"
    "s_mov_b64 exec, -1\n"
    "s_waitcnt vmcnt(0)\n"
    "s_cbranch_scc1 .Lk_L0A\n"
    ".Lk_L0AR:\n"
    "s_movk_i32 s39, 0x4000\n"
    ".Lk_P_Q:\n"
    "global_load_dwordx4 v[2:5], v11, s[42:43] sc1\n"
    "s_waitcnt vmcnt(0)\n"
    "v_add_f32_dpp v2, v2, v2 quad_perm:[1,0,3,2] row_mask:0xf bank_mask:0xf\n"
    "v_add_f32_dpp v4, v4, v4 quad_perm:[1,0,3,2] row_mask:0xf bank_mask:0xf\n"
    "v_xor_b32 v3, v3, v37\n"
    "v_add_f32_dpp v2, v2, v2 quad_perm:[2,3,0,1] row_mask:0xf bank_mask:0xf\n"
    "v_add_f32_dpp v4, v4, v4 quad_perm:[2,3,0,1] row_mask:0xf bank_mask:0xf\n"
    "v_xor_b32 v5, v5, v37\n"
    "v_cvt_pk_bf16_f32 v2, v2, v4\n"
    "ds_write_b32 v12, v2 offset:0\n"
    "v_or_b32 v3, v3, v5\n"
    "v_cmp_ne_u32 vcc, 0, v3\n"
    "s_cbranch_vccnz .Lk_R_Q\n"
    ".Lk_D_Q:\n"
    "s_cmp_eq_u32 s37, 0\n"
    "s_cbranch_scc1 .Lk_L0D\n"
    ".Lk_L0DR:\n"
    "s_waitcnt lgkmcnt(0)\n"
    "s_barrier\n"
    "s_cmp_eq_u32 s37, 0\n"
    "s_cbranch_scc1 .Lk_L0B\n"
    ".Lk_MERGE:\n"
    "v_mov_b32 v164, 0\n"
    "v_mov_b32 v165, 0\n"
    "v_mov_b32 v166, 0\n"
    "v_mov_b32 v167, 0\n"
    "s_cmp_lt_u32 s34, 2\n"
    "s_cbranch_scc0 .Lk_B2F\n"
    "s_lshl_b32 s46, s37, 12\n"
    "v_lshl_add_u32 v1, v0, 3, s46\n"
    "v_lshlrev_b32 v2, 3, v0\n"
    "s_setprio 0\n"
    "s_mov_b32 s39, 0x100000\n"
    ".Lk_P_M:\n"
    "ds_read2st64_b64 v[16:19], v1 offset0:1 offset1:2\n"
    "ds_read2st64_b64 v[20:23], v1 offset0:3 offset1:4\n"
    "ds_read2st64_b64 v[24:27], v1 offset0:5 offset1:6\n"
    "ds_read2st64_b64 v[28:31], v1 offset0:7 offset1:8\n"
    "s_waitcnt lgkmcnt(0)\n"
    "v_max3_u32 v3, v17, v19, v21\n"
    "v_max3_u32 v3, v3, v23, v25\n"
    "v_max3_u32 v3, v3, v27, v29\n"
    "v_max_u32 v3, v3, v31\n"
    "v_cmp_eq_u32 vcc, -1, v3\n"
    "s_cbranch_vccnz .Lk_R_M\n"
    ".Lk_D_M:\n"
    "s_setprio 3\n"
    "v_max3_f32 v5, v17, v19, v21\n"
    "v_max3_f32 v5, v5, v23, v25\n"
    "v_max3_f32 v5, v5, v27, v29\n"
    "v_max_f32 v5, v5, v31\n"
    "v_sub_f32 v152, v17, v5\n"
    "v_sub_f32 v153, v19, v5\n"
    "v_sub_f32 v154, v21, v5\n"
    "v_sub_f32 v155, v23, v5\n"
    "v_sub_f32 v156, v25, v5\n"
    "v_sub_f32 v157, v27, v5\n"
    "v_sub_f32 v158, v29, v5\n"
    "v_sub_f32 v159, v31, v5\n"
    "v_exp_f32 v152, v152\n"
    "v_exp_f32 v153, v153\n"
    "v_exp_f32 v154, v154\n"
    "v_exp_f32 v155, v155\n"
    "v_exp_f32 v156, v156\n"
    "v_exp_f32 v157, v157\n"
    "v_exp_f32 v158, v158\n"
    "v_exp_f32 v159, v159\n"
    "s_cmp_eq_u32 s34, 0\n"
    "s_cbranch_scc1 .Lk_B2\n"
    "v_mul_f32 v4, v16, v152\n"
    "v_fmac_f32 v4, v18, v153\n"
    "v_fmac_f32 v4, v20, v154\n"
    "v_fmac_f32 v4, v22, v155\n"
    "v_fmac_f32 v4, v24, v156\n"
    "v_fmac_f32 v4, v26, v157\n"
    "v_fmac_f32 v4, v28, v158\n"
    "v_fmac_f32 v4, v30, v159\n"
    "s_nop 1\n"
    "v_add_f32_dpp v4, v4, v4 quad_perm:[1,0,3,2] row_mask:0xf bank_mask:0xf\n"
    "s_nop 1\n"
    "v_add_f32_dpp v4, v4, v4 quad_perm:[2,3,0,1] row_mask:0xf bank_mask:0xf\n"
    "s_nop 1\n"
    "v_add_f32_dpp v4, v4, v4 row_ror:4 row_mask:0xf bank_mask:0xf\n"
    "s_nop 1\n"
    "v_add_f32_dpp v4, v4, v4 row_ror:8 row_mask:0xf bank_mask:0xf\n"
    "s_nop 1\n"
    "v_add_f32_dpp v4, v4, v4 row_bcast:15 row_mask:0xa bank_mask:0xf\n"
    "s_nop 1\n"
    "v_add_f32_dpp v4, v4, v4 row_bcast:31 row_mask:0xc bank_mask:0xf\n"
    "s_mov_b32 exec_lo, 0\n"
    "s_mov_b32 exec_hi, 0x80000000\n"
    "global_store_dwordx2 v13, v[4:5], s[40:41] offset:0 sc1\n"
    "global_store_dwordx2 v13, v[4:5], s[52:53] offset:0 sc1\n"
    "s_mov_b64 exec, -1\n"
    ".Lk_B2:\n"
    "s_cmp_eq_u32 s34, 0\n"
    "s_barrier\n"
    "s_cbranch_scc0 .Lk_ROLE\n"
    "ds_read2st64_b64 v[16:19], v2 offset0:17 offset1:18\n"
    "ds_read2st64_b64 v[20:23], v2 offset0:19 offset1:20\n"
    "ds_read2st64_b64 v[24:27], v2 offset0:21 offset1:22\n"
    "ds_read2st64_b64 v[28:31], v2 offset0:23 offset1:24\n"
    "s_waitcnt lgkmcnt(3)\n"
    "v_pk_mul_f32 v[4:5], v[16:17], v[152:153] op_sel:[0,0] op_sel_hi:[1,0]\n"
    "v_pk_fma_f32 v[4:5], v[18:19], v[152:153], v[4:5] op_sel:[0,1,0] op_sel_hi:[1,1,1]\n"
    "s_waitcnt lgkmcnt(2)\n"
    "v_pk_fma_f32 v[4:5], v[20:21], v[154:155], v[4:5] op_sel:[0,0,0] op_sel_hi:[1,0,1]\n"
    "v_pk_fma_f32 v[4:5], v[22:23], v[154:155], v[4:5] op_sel:[0,1,0] op_sel_hi:[1,1,1]\n"
    "s_waitcnt lgkmcnt(1)\n"
    "v_pk_fma_f32 v[4:5], v[24:25], v[156:157], v[4:5] op_sel:[0,0,0] op_sel_hi:[1,0,1]\n"
    "v_pk_fma_f32 v[4:5], v[26:27], v[156:157], v[4:5] op_sel:[0,1,0] op_sel_hi:[1,1,1]\n"
    "s_waitcnt lgkmcnt(0)\n"
    "v_pk_fma_f32 v[4:5], v[28:29], v[158:159], v[4:5] op_sel:[0,0,0] op_sel_hi:[1,0,1]\n"
    "v_pk_fma_f32 v[4:5], v[30:31], v[158:159], v[4:5] op_sel:[0,1,0] op_sel_hi:[1,1,1]\n"
    "v_cvt_pk_bf16_f32 v4, v4, v5\n"
    "global_store_dword v13, v4, s[40:41] sc1\n"
    "global_store_dword v13, v4, s[52:53] sc1\n"
    ".Lk_ROLE:\n"
    "s_cmp_eq_u32 s37, 0\n"
    "s_cbranch_scc1 .Lk_L0C\n"
    ".Lk_SR2:\n"
    "s_cmp_eq_u32 s35, 0\n"
    "s_cbranch_scc0 .Lk_ROLE1\n"
    "s_sleep 12\n"
    "s_movk_i32 s39, 0x4000\n"
    ".Lk_P_W:\n"
    "global_load_dwordx2 v[2:3], v163, s[42:43] offset:2048 sc1\n"
    "global_load_dwordx4 v[124:127], v14, s[42:43] offset:0 sc1\n"
    "global_load_dwordx4 v[128:131], v14, s[42:43] offset:512 sc1\n"
    "global_load_dwordx4 v[132:135], v14, s[42:43] offset:1024 sc1\n"
    "global_load_dwordx4 v[136:139], v14, s[42:43] offset:1536 sc1\n"
    "s_waitcnt vmcnt(0)\n"
    "v_smfmac_f32_16x16x64_bf16 v[164:167], v[124:127], v[60:67], v160\n"
    "v_max_f32_dpp v4, v3, v3 quad_perm:[1,0,3,2] row_mask:0xf bank_mask:0xf\n"
    "v_max3_u32 v1, v2, v3, v124\n"
    "v_max3_u32 v1, v1, v125, v126\n"
    "v_smfmac_f32_16x16x64_bf16 v[164:167], v[128:131], v[68:75], v160\n"
    "v_max_f32_dpp v4, v4, v4 quad_perm:[2,3,0,1] row_mask:0xf bank_mask:0xf\n"
    "v_max3_u32 v1, v1, v127, v128\n"
    "v_max3_u32 v1, v1, v129, v130\n"
    "v_max_f32_dpp v4, v4, v4 row_half_mirror row_mask:0xf bank_mask:0xf\n"
    "v_smfmac_f32_16x16x64_bf16 v[164:167], v[132:135], v[76:83], v160\n"
    "v_sub_f32 v4, v3, v4\n"
    "v_exp_f32 v4, v4\n"
    "v_max3_u32 v1, v1, v131, v132\n"
    "v_mul_f32 v5, v2, v4\n"
    "v_smfmac_f32_16x16x64_bf16 v[164:167], v[136:139], v[84:91], v160\n"
    "v_max3_u32 v1, v1, v133, v134\n"
    "v_add_f32_dpp v5, v5, v5 quad_perm:[1,0,3,2] row_mask:0xf bank_mask:0xf\n"
    "v_max3_u32 v1, v1, v135, v136\n"
    "v_max3_u32 v1, v1, v137, v138\n"
    "v_add_f32_dpp v5, v5, v5 quad_perm:[2,3,0,1] row_mask:0xf bank_mask:0xf\n"
    "v_max_u32 v1, v1, v139\n"
    "v_cmp_eq_u32 vcc, -1, v1\n"
    "s_cbranch_vccnz .Lk_R_W\n"
    ".Lk_D_W:\n"
    "v_add_f32_dpp v5, v5, v5 row_half_mirror row_mask:0xf bank_mask:0xf\n"
    "v_mul_f32_dpp v6, v4, v164 row_newbcast:0 row_mask:0xf bank_mask:0xf\n"
    "v_rcp_f32 v5, v5\n"
    "v_mul_f32_dpp v7, v4, v165 row_newbcast:0 row_mask:0xf bank_mask:0xf\n"
    "v_fmac_f32_dpp v6, v4, v166 row_newbcast:1 row_mask:0xf bank_mask:0xf\n"
    "s_nop 0\n"
    "v_fmac_f32_dpp v7, v4, v167 row_newbcast:1 row_mask:0xf bank_mask:0xf\n"
    "s_nop 1\n"
    "v_permlane32_swap_b32 v6, v7\n"
    "v_add_f32 v6, v6, v7\n"
    "v_mul_f32 v6, v6, v5\n"
    "ds_write_b32 v161, v6 offset:12800\n"
    "s_cmp_eq_u32 s34, 0\n"
    "s_waitcnt lgkmcnt(0)\n"
    "s_barrier\n"
    "s_cbranch_scc0 .Lk_NEXT\n"
    "ds_read2_b32 v[2:3], v162 offset0:0 offset1:32\n"
    "ds_read2_b32 v[4:5], v162 offset0:64 offset1:96\n"
    "ds_read2_b32 v[6:7], v162 offset0:128 offset1:160\n"
    "ds_read2_b32 v[8:9], v162 offset0:192 offset1:224\n"
    "s_waitcnt lgkmcnt(0)\n"
    "v_pk_add_f32 v[2:3], v[2:3], v[4:5]\n"
    "v_pk_add_f32 v[6:7], v[6:7], v[8:9]\n"
    "v_pk_add_f32 v[2:3], v[2:3], v[6:7]\n"
    "v_add_f32 v2, v2, v3\n"
    "v_cvt_pk_bf16_f32 v2, v2, v2\n"
    "global_store_short v15, v2, s[40:41] sc1\n"
    "s_branch .Lk_NEXT\n"
    ".Lk_ROLE1:\n"
    "s_cmp_eq_u32 s35, 1\n"
    "s_cbranch_scc0 .Lk_ROLE2\n"
    "s_cmp_eq_u32 s37, 0\n"
    "s_cbranch_scc0 .Lk_M1L1\n"
    "s_mov_b32 exec_lo, 0x03ff03ff\n"
    "s_mov_b32 exec_hi, 0x03ff03ff\n"
    "v_lshlrev_b32 v1, 1, v163\n"
    "global_load_dwordx2 v[110:111], v1, s[8:9]\n"
    "s_waitcnt vmcnt(0)\n"
    "s_mov_b64 exec, -1\n"
    "v_lshlrev_b32 v114, 16, v115\n"
    "v_and_b32 v115, s49, v115\n"
    "s_add_u32 s46, s34, 6\n"
    "s_lshl_b32 s46, s46, 14\n"
    "s_lshl_b32 s47, s36, 5\n"
    "s_add_u32 s46, s46, s47\n"
    "s_xor_b32 s47, s59, 0x2000\n"
    "s_add_u32 s46, s46, s47\n"
    "s_add_u32 s46, s46, 0xc0000\n"
    "v_and_b32 v1, 3, v0\n"
    "v_mul_u32_u24 v117, 0x5555, v1\n"
    "v_lshl_add_u32 v118, v1, 3, s46\n"
    "v_bfe_u32 v1, v0, 2, 2\n"
    "v_lshl_add_u32 v118, v1, 11, v118\n"
    "v_and_b32 v119, 48, v0\n"
    "v_lshlrev_b32 v120, 2, v0\n"
    "s_add_u32 s44, s34, 6\n"
    "s_min_u32 s44, s44, 9\n"
    "s_lshl_b32 s47, s44, 12\n"
    "s_add_u32 s47, s47, 14592\n"
    "v_lshrrev_b32 v1, 4, v0\n"
    "v_lshl_add_u32 v1, v1, 9, s47\n"
    "v_and_b32 v140, 15, v0\n"
    "v_lshl_add_u32 v1, v140, 3, v1\n"
    "ds_read2_b32 v[140:141], v1 offset0:0 offset1:32\n"
    "ds_read2_b32 v[142:143], v1 offset0:64 offset1:96\n"
    "ds_read2_b32 v[148:149], v1 offset0:1 offset1:33\n"
    "ds_read2_b32 v[150:151], v1 offset0:65 offset1:97\n"
    "v_add_u32 v1, 0x800, v1\n"
    "ds_read2_b32 v[144:145], v1 offset0:0 offset1:32\n"
    "ds_read2_b32 v[146:147], v1 offset0:64 offset1:96\n"
    "ds_read2_b32 v[152:153], v1 offset0:1 offset1:33\n"
    "ds_read2_b32 v[154:155], v1 offset0:65 offset1:97\n"
    ".Lk_G_Z0:\n"
    "s_memrealtime s[44:45]\n"
    "s_waitcnt lgkmcnt(0)\n"
    "s_sub_u32 s44, s44, s62\n"
    "s_cmp_lt_u32 s44, 100\n"
    "s_cbranch_scc1 .Lk_G_Z0\n"
    "s_branch .Lk_M1GO\n"
    ".Lk_M1L1:\n"
    "s_add_u32 s44, s34, 6\n"
    "s_min_u32 s44, s44, 9\n"
    "s_lshl_b32 s47, s44, 12\n"
    "s_add_u32 s47, s47, 14592\n"
    "v_lshrrev_b32 v1, 4, v0\n"
    "v_lshl_add_u32 v1, v1, 9, s47\n"
    "v_and_b32 v140, 15, v0\n"
    "v_lshl_add_u32 v1, v140, 3, v1\n"
    "ds_read2_b32 v[140:141], v1 offset0:0 offset1:32\n"
    "ds_read2_b32 v[142:143], v1 offset0:64 offset1:96\n"
    "ds_read2_b32 v[148:149], v1 offset0:1 offset1:33\n"
    "ds_read2_b32 v[150:151], v1 offset0:65 offset1:97\n"
    "v_add_u32 v1, 0x800, v1\n"
    "ds_read2_b32 v[144:145], v1 offset0:0 offset1:32\n"
    "ds_read2_b32 v[146:147], v1 offset0:64 offset1:96\n"
    "ds_read2_b32 v[152:153], v1 offset0:1 offset1:33\n"
    "ds_read2_b32 v[154:155], v1 offset0:65 offset1:97\n"
    "s_mov_b32 exec_lo, 0x03ff03ff\n"
    "s_mov_b32 exec_hi, 0x03ff03ff\n"
    "v_lshrrev_b32 v1, 4, v163\n"
    "v_lshlrev_b32 v1, 5, v1\n"
    "v_and_b32 v2, 15, v163\n"
    "v_lshl_add_u32 v1, v2, 1, v1\n"
    "global_load_dwordx2 v[110:111], v1, s[50:51] sc1\n"
    ".Lk_G_Z:\n"
    "s_memrealtime s[44:45]\n"
    "s_waitcnt lgkmcnt(0)\n"
    "s_sub_u32 s44, s44, s62\n"
    "s_cmp_lt_u32 s44, 395\n"
    "s_cbranch_scc1 .Lk_G_Z\n"
    "s_waitcnt vmcnt(0)\n"
    ".Lk_M1GO:\n"
    "s_mov_b32 exec_lo, 0x03ff03ff\n"
    "s_mov_b32 exec_hi, 0x03ff03ff\n"
    "v_lshlrev_b32 v108, 16, v110\n"
    "v_and_b32 v109, s49, v110\n"
    "v_lshlrev_b32 v110, 16, v111\n"
    "v_and_b32 v111, s49, v111\n"
    "s_movk_i32 s39, 0x4000\n"
    ".Lk_P_Z:\n"
    "global_load_dwordx4 v[124:127], v14, s[42:43] offset:-2560 sc1\n"
    "global_load_dwordx4 v[128:131], v14, s[42:43] offset:-1920 sc1\n"
    "global_load_dwordx4 v[132:135], v14, s[42:43] offset:-1280 sc1\n"
    "global_load_dwordx4 v[136:139], v14, s[42:43] offset:-640 sc1\n"
    "s_waitcnt vmcnt(3)\n"
    "v_dot2_f32_bf16 v4, v124, s48, v108\n"
    "v_dot2_f32_bf16 v5, v125, s48, v109\n"
    "v_dot2_f32_bf16 v6, v126, s48, v110\n"
    "v_dot2_f32_bf16 v7, v127, s48, v111\n"
    "s_waitcnt vmcnt(2)\n"
    "v_dot2_f32_bf16 v4, v128, s48, v4\n"
    "v_dot2_f32_bf16 v5, v129, s48, v5\n"
    "v_dot2_f32_bf16 v6, v130, s48, v6\n"
    "v_dot2_f32_bf16 v7, v131, s48, v7\n"
    "s_waitcnt vmcnt(1)\n"
    "v_dot2_f32_bf16 v4, v132, s48, v4\n"
    "v_dot2_f32_bf16 v5, v133, s48, v5\n"
    "v_dot2_f32_bf16 v6, v134, s48, v6\n"
    "v_dot2_f32_bf16 v7, v135, s48, v7\n"
    "s_waitcnt vmcnt(0)\n"
    "v_dot2_f32_bf16 v4, v136, s48, v4\n"
    "v_dot2_f32_bf16 v5, v137, s48, v5\n"
    "v_dot2_f32_bf16 v6, v138, s48, v6\n"
    "v_dot2_f32_bf16 v7, v139, s48, v7\n"
    "s_nop 0\n"
    "v_cvt_pk_bf16_f32 v112, v4, v5\n"
    "s_nop 0\n"
    "v_cvt_pk_bf16_f32 v113, v6, v7\n"
    "s_mov_b64 exec, -1\n"
    "v_dot2_f32_bf16 v2, v112, v112, 0\n"
    "v_dot2_f32_bf16 v2, v113, v113, v2\n"
    "v_mov_b32_dpp v100, v112 row_newbcast:0 row_mask:0xf bank_mask:0x1\n"
    "v_mov_b32_dpp v101, v113 row_newbcast:0 row_mask:0xf bank_mask:0x1\n"
    "v_mov_b32_dpp v102, v112 row_newbcast:1 row_mask:0xf bank_mask:0x1\n"
    "v_mov_b32_dpp v103, v113 row_newbcast:1 row_mask:0xf bank_mask:0x1\n"
    "v_cmp_u_f32 vcc, v2, v2\n"
    "s_cbranch_vccnz .Lk_R_Z\n"
    ".Lk_D_Z:\n"
    "v_smfmac_f32_16x16x64_bf16 v[164:167], v[100:103], v[60:67], v160\n"
    "v_mov_b32_dpp v104, v112 row_newbcast:2 row_mask:0xf bank_mask:0x1\n"
    "v_mov_b32_dpp v105, v113 row_newbcast:2 row_mask:0xf bank_mask:0x1\n"
    "v_mov_b32_dpp v106, v112 row_newbcast:3 row_mask:0xf bank_mask:0x1\n"
    "v_mov_b32_dpp v107, v113 row_newbcast:3 row_mask:0xf bank_mask:0x1\n"
    "v_dot2_f32_bf16 v1, v112, s48, 0\n"
    "v_dot2_f32_bf16 v1, v113, s48, v1\n"
    "v_smfmac_f32_16x16x64_bf16 v[164:167], v[104:107], v[68:75], v160\n"
    "v_mov_b32_dpp v100, v112 row_newbcast:4 row_mask:0xf bank_mask:0x1\n"
    "v_mov_b32_dpp v101, v113 row_newbcast:4 row_mask:0xf bank_mask:0x1\n"
    "v_mov_b32_dpp v102, v112 row_newbcast:5 row_mask:0xf bank_mask:0x1\n"
    "v_mov_b32_dpp v103, v113 row_newbcast:5 row_mask:0xf bank_mask:0x1\n"
    "v_permlane32_swap_b32 v1, v2\n"
    "v_add_f32 v1, v1, v2\n"
    "v_smfmac_f32_16x16x64_bf16 v[164:167], v[100:103], v[76:83], v160\n"
    "v_mov_b32_dpp v104, v112 row_newbcast:6 row_mask:0xf bank_mask:0x1\n"
    "v_mov_b32_dpp v105, v113 row_newbcast:6 row_mask:0xf bank_mask:0x1\n"
    "v_add_f32_dpp v1, v1, v1 quad_perm:[1,0,3,2] row_mask:0xf bank_mask:0xf\n"
    "v_mov_b32_dpp v106, v112 row_newbcast:7 row_mask:0xf bank_mask:0x1\n"
    "v_mov_b32_dpp v107, v113 row_newbcast:7 row_mask:0xf bank_mask:0x1\n"
    "v_add_f32_dpp v1, v1, v1 quad_perm:[2,3,0,1] row_mask:0xf bank_mask:0xf\n"
    "s_nop 0\n"
    "v_smfmac_f32_16x16x64_bf16 v[164:167], v[104:107], v[84:91], v160\n"
    "v_mov_b32_dpp v100, v112 row_newbcast:8 row_mask:0xf bank_mask:0x1\n"
    "v_mov_b32_dpp v101, v113 row_newbcast:8 row_mask:0xf bank_mask:0x1\n"
    "v_add_f32_dpp v1, v1, v1 row_ror:4 row_mask:0xf bank_mask:0xf\n"
    "v_mov_b32_dpp v102, v112 row_newbcast:9 row_mask:0xf bank_mask:0x1\n"
    "v_mov_b32_dpp v103, v113 row_newbcast:9 row_mask:0xf bank_mask:0x1\n"
    "v_add_f32_dpp v1, v1, v1 row_ror:8 row_mask:0xf bank_mask:0xf\n"
    "s_nop 0\n"
    "v_smfmac_f32_16x16x64_bf16 v[164:167], v[100:103], v[92:99], v160\n"
    "v_add_f32_dpp v1, v1, v1 row_bcast:15 row_mask:0xa bank_mask:0xf\n"
    "ds_write_b32 v116, v1\n"
    "s_cmp_eq_u32 s34, 0\n"
    "s_nop 4\n"
    "ds_write_b64 v161, v[164:165]\n"
    "s_waitcnt lgkmcnt(0)\n"
    "s_barrier\n"
    "s_cbranch_scc0 .Lk_M1P\n"
    "v_mov_b32 v1, 0\n"
    "ds_read_b128 v[16:19], v1 offset:13824\n"
    "ds_read_b128 v[24:27], v1 offset:13840\n"
    "ds_read_b128 v[124:127], v162 offset:12800\n"
    "ds_read_b128 v[128:131], v162 offset:13056\n"
    "ds_read_b128 v[132:135], v162 offset:13312\n"
    "ds_read_b128 v[136:139], v162 offset:13568\n"
    "v_mov_b32 v9, 0x2edbe6ff\n"
    "s_waitcnt lgkmcnt(4)\n"
    "v_pk_add_f32 v[16:17], v[16:17], v[18:19]\n"
    "v_pk_add_f32 v[24:25], v[24:25], v[26:27]\n"
    "v_pk_add_f32 v[16:17], v[16:17], v[24:25]\n"
    "v_pk_mul_f32 v[16:17], v[16:17], s[54:55] op_sel_hi:[1,0]\n"
    "v_fma_f32 v2, -v16, v16, v17\n"
    "v_fma_f32 v2, |v2|, s58, v9\n"
    "v_rsq_f32 v2, v2\n"
    "s_waitcnt lgkmcnt(0)\n"
    "s_memrealtime s[44:45]\n"
    "v_pk_add_f32 v[124:125], v[124:125], v[128:129]\n"
    "v_pk_add_f32 v[126:127], v[126:127], v[130:131]\n"
    "v_pk_add_f32 v[132:133], v[132:133], v[136:137]\n"
    "v_pk_add_f32 v[134:135], v[134:135], v[138:139]\n"
    "v_pk_add_f32 v[124:125], v[124:125], v[132:133]\n"
    "v_pk_add_f32 v[126:127], v[126:127], v[134:135]\n"
    "v_pk_fma_f32 v[4:5], v[126:127], v[16:17], v[124:125] op_sel_hi:[1,0,1] neg_lo:[0,1,0] neg_hi:[0,1,0]\n"
    "v_pk_fma_f32 v[4:5], v[4:5], v[2:3], v[114:115] op_sel_hi:[1,0,1]\n"
    "v_pk_mul_f32 v[6:7], v[4:5], v[4:5]\n"
    "v_pk_fma_f32 v[6:7], v[6:7], s[56:57], s[56:57] op_sel:[0,0,1] op_sel_hi:[1,0,1]\n"
    "v_pk_mul_f32 v[6:7], v[6:7], v[4:5]\n"
    "v_exp_f32 v6, v6\n"
    "v_exp_f32 v7, v7\n"
    "s_nop 0\n"
    "v_pk_add_f32 v[6:7], v[6:7], 1.0 op_sel_hi:[1,0]\n"
    "v_rcp_f32 v6, v6\n"
    "v_rcp_f32 v7, v7\n"
    "s_nop 0\n"
    "v_pk_mul_f32 v[4:5], v[4:5], v[6:7]\n"
    "s_mov_b64 exec, 0xffff\n"
    "v_cvt_pk_bf16_f32 v4, v4, v5\n"
    "ds_write_b32 v120, v4 offset:14464\n"
    "s_mov_b64 exec, -1\n"
    "s_waitcnt lgkmcnt(0)\n"
    "s_barrier\n"
    "s_add_u32 s46, s30, s37\n"
    "s_cmp_eq_u32 s46, 160\n"
    "s_cbranch_scc0 .Lk_NEXT\n"
    "v_lshlrev_b32 v6, 13, v0\n"
    "v_add_u32 v6, 0x54800, v6\n"
    "s_waitcnt lgkmcnt(0)\n"
    "s_andn2_b32 s44, s44, 1\n"
    "v_mov_b32 v7, s44\n"
    "s_mov_b64 exec, 0xff\n"
    "global_store_dword v6, v7, s[28:29] sc1\n"
    "s_mov_b64 exec, -1\n"
    "s_branch .Lk_NEXT\n"
    ".Lk_M1P:\n"
    "v_mov_b32 v128, 0\n"
    "v_mov_b32 v129, 0\n"
    "v_mov_b32 v130, 0\n"
    "v_mov_b32 v131, 0\n"
    "v_mov_b32 v132, 0\n"
    "v_mov_b32 v133, 0\n"
    "v_mov_b32 v134, 0\n"
    "v_mov_b32 v135, 0\n"
    "s_cmp_eq_u32 s34, 1\n"
    "s_barrier\n"
    "s_cbranch_scc1 .Lk_NEXT\n"
    "ds_read_b128 v[124:127], v119 offset:14464\n"
    "s_waitcnt lgkmcnt(0)\n"
    "v_smfmac_f32_16x16x64_bf16 v[128:131], v[124:127], v[140:147], v160\n"
    "v_smfmac_f32_16x16x64_bf16 v[132:135], v[124:127], v[148:155], v160\n"
    "s_mov_b64 exec, 0xffff\n"
    "s_nop 5\n"
    "v_cvt_pk_bf16_f32 v128, v128, v129\n"
    "v_cvt_pk_bf16_f32 v129, v132, v133\n"
    "global_store_dwordx2 v118, v[128:129], s[40:41] sc1\n"
    "s_mov_b64 exec, -1\n"
    "s_branch .Lk_NEXT\n"
    ".Lk_ROLE2:\n"
    "s_cmp_eq_u32 s35, 2\n"
    "s_cbranch_scc1 .Lk_ROLEM2\n"
    "s_barrier\n"
    "s_branch .Lk_NEXT\n"
    ".Lk_ROLEM2:\n"
    "s_cmp_eq_u32 s34, 0\n"
    "s_barrier\n"
    "s_cbranch_scc0 .Lk_NEXT\n"
    "s_cmp_eq_u32 s37, 0\n"
    "s_cbranch_scc0 .Lk_M2L1\n"
    "s_lshr_b32 s46, s36, 2\n"
    "s_lshl_b32 s46, s46, 14\n"
    "s_and_b32 s47, s36, 3\n"
    "s_lshl_b32 s47, s47, 11\n"
    "s_add_u32 s46, s46, s47\n"
    "s_cmp_ge_u32 s36, 20\n"
    "s_cselect_b32 s47, 0x2000, 0\n"
    "s_xor_b32 s47, s47, s59\n"
    "s_add_u32 s46, s46, s47\n"
    "s_add_u32 s46, s46, 0xc0000\n"
    "v_lshl_add_u32 v14, v0, 3, s46\n"
    "s_waitcnt vmcnt(0)\n"
    "v_lshlrev_b32 v60, 16, v62\n"
    "v_and_b32 v61, s49, v62\n"
    "v_lshlrev_b32 v62, 16, v63\n"
    "v_and_b32 v63, s49, v63\n"
    ".Lk_G_Y0:\n"
    "s_memrealtime s[44:45]\n"
    "s_waitcnt lgkmcnt(0)\n"
    "s_sub_u32 s44, s44, s62\n"
    "s_cmp_lt_u32 s44, 110\n"
    "s_cbranch_scc1 .Lk_G_Y0\n"
    "s_branch .Lk_M2L0\n"
    ".Lk_M2L1:\n"
    ".Lk_G_Y:\n"
    "s_memrealtime s[44:45]\n"
    "s_waitcnt lgkmcnt(0)\n"
    "s_sub_u32 s44, s44, s62\n"
    "s_cmp_lt_u32 s44, 405\n"
    "s_cbranch_scc1 .Lk_G_Y\n"
    ".Lk_M2L0:\n"
    "v_lshlrev_b32 v6, 16, v64\n"
    "v_and_b32 v7, s49, v64\n"
    "v_lshlrev_b32 v8, 16, v65\n"
    "v_and_b32 v9, s49, v65\n"
    "s_movk_i32 s39, 0x4000\n"
    ".Lk_P_Y:\n"
    "global_load_dwordx4 v[2:5], v120, s[42:43] sc1\n"
    "s_waitcnt vmcnt(0)\n"
    "v_dot2_f32_bf16 v2, v2, s48, 0\n"
    "v_dot2_f32_bf16 v3, v3, s48, 0\n"
    "v_dot2_f32_bf16 v4, v4, s48, 0\n"
    "v_dot2_f32_bf16 v5, v5, s48, 0\n"
    "s_nop 0\n"
    "v_add_f32 v1, v2, v3\n"
    "s_nop 0\n"
    "v_add_f32 v24, v4, v5\n"
    "v_cmp_u_f32 vcc, v1, v24\n"
    "s_cbranch_vccnz .Lk_R_Y\n"
    ".Lk_D_Y:\n"
    "s_memrealtime s[98:99]\n"
    "v_add_f32_dpp v2, v2, v2 row_ror:4 row_mask:0xf bank_mask:0xf\n"
    "v_add_f32_dpp v3, v3, v3 row_ror:4 row_mask:0xf bank_mask:0xf\n"
    "v_add_f32_dpp v4, v4, v4 row_ror:4 row_mask:0xf bank_mask:0xf\n"
    "v_add_f32_dpp v5, v5, v5 row_ror:4 row_mask:0xf bank_mask:0xf\n"
    "v_add_f32_dpp v2, v2, v2 row_ror:8 row_mask:0xf bank_mask:0xf\n"
    "v_add_f32_dpp v3, v3, v3 row_ror:8 row_mask:0xf bank_mask:0xf\n"
    "v_add_f32_dpp v4, v4, v4 row_ror:8 row_mask:0xf bank_mask:0xf\n"
    "v_add_f32_dpp v5, v5, v5 row_ror:8 row_mask:0xf bank_mask:0xf\n"
    "v_pk_add_f32 v[2:3], v[2:3], v[6:7]\n"
    "v_pk_add_f32 v[4:5], v[4:5], v[8:9]\n"
    "v_cvt_pk_bf16_f32 v2, 0, v2\n"
    "v_mov_b32 v72, 0\n"
    "v_cvt_pk_bf16_f32 v3, 0, v3\n"
    "v_mov_b32 v73, 0\n"
    "v_cvt_pk_bf16_f32 v4, 0, v4\n"
    "v_mov_b32 v74, 0\n"
    "v_cvt_pk_bf16_f32 v5, 0, v5\n"
    "v_mov_b32 v75, 0\n"
    "s_mov_b32 exec_lo, 0x000f000f\n"
    "s_mov_b32 exec_hi, 0x000f000f\n"
    "v_pk_add_f32 v[72:73], v[2:3], v[60:61]\n"
    "v_pk_add_f32 v[74:75], v[4:5], v[62:63]\n"
    "s_mov_b64 exec, -1\n"
    "s_cmp_eq_u32 s37, 0\n"
    "s_movk_i32 s46, 70\n"
    "s_cmovk_i32 s46, 70\n"
    ".Lk_G_P:\n"
    "s_memrealtime s[44:45]\n"
    "s_waitcnt lgkmcnt(0)\n"
    "s_sub_u32 s44, s44, s98\n"
    "s_cmp_lt_u32 s44, s46\n"
    "s_cbranch_scc1 .Lk_G_P\n"
    "s_movk_i32 s39, 0x4000\n"
    ".Lk_P_H:\n"
    "global_load_dwordx2 v[124:125], v14, s[40:41] offset:0 sc1\n"
    "global_load_dwordx2 v[126:127], v14, s[40:41] offset:512 sc1\n"
    "global_load_dwordx2 v[128:129], v14, s[40:41] offset:1024 sc1\n"
    "s_mov_b64 exec, 0xffffff\n"
    "global_load_dwordx2 v[130:131], v14, s[40:41] offset:1536 sc1\n"
    "s_mov_b64 exec, -1\n"
    "v_mov_b32 v164, v72\n"
    "v_mov_b32 v165, v73\n"
    "v_mov_b32 v166, v74\n"
    "v_mov_b32 v167, v75\n"
    "s_waitcnt vmcnt(0)\n"
    "s_nop 0\n"
    "v_smfmac_f32_16x16x64_bf16 v[164:167], v[68:71], v[124:131], v160\n"
    "s_nop 7\n"
    "v_cmp_u_f32 vcc, v164, v166\n"
    "v_add_f32_dpp v164, v164, v164 row_ror:4 row_mask:0xf bank_mask:0xf\n"
    "v_add_f32_dpp v165, v165, v165 row_ror:4 row_mask:0xf bank_mask:0xf\n"
    "v_add_f32_dpp v166, v166, v166 row_ror:4 row_mask:0xf bank_mask:0xf\n"
    "v_add_f32_dpp v167, v167, v167 row_ror:4 row_mask:0xf bank_mask:0xf\n"
    "s_cbranch_vccnz .Lk_R_H\n"
    ".Lk_D_H:\n"
    "v_add_f32_dpp v164, v164, v164 row_ror:8 row_mask:0xf bank_mask:0xf\n"
    "v_add_f32_dpp v165, v165, v165 row_ror:8 row_mask:0xf bank_mask:0xf\n"
    "v_add_f32_dpp v166, v166, v166 row_ror:8 row_mask:0xf bank_mask:0xf\n"
    "v_add_f32_dpp v167, v167, v167 row_ror:8 row_mask:0xf bank_mask:0xf\n"
    "s_cmp_eq_u32 s37, 0\n"
    "v_cvt_pk_bf16_f32 v64, v164, v165\n"
    "v_cvt_pk_bf16_f32 v65, v166, v167\n"
    "s_cbranch_scc1 .Lk_M2X0\n"
    "s_mov_b64 exec, 0xf\n"
    "global_store_dwordx2 v121, v[64:65], s[26:27]\n"
    "s_endpgm\n"
    ".Lk_M2X0:\n"
    "s_mov_b32 exec_hi, 0\n"
    "global_store_dwordx2 v15, v[64:65], s[40:41] sc1\n"
    "s_mov_b64 exec, -1\n"
    ".Lk_NEXT:\n"
    "s_cmp_lg_u32 s37, 0\n"
    "s_cbranch_scc1 .Lk_HEND\n"
    "s_mov_b32 s37, 1\n"
    "s_add_u32 s40, s40, 0x100000\n"
    "s_addc_u32 s41, s41, 0\n"
    "s_add_u32 s52, s52, 0x100000\n"
    "s_addc_u32 s53, s53, 0\n"
    "s_add_u32 s42, s42, 0x100000\n"
    "s_addc_u32 s43, s43, 0\n"
    "s_branch .Lk_HLOOP\n"
    ".Lk_HEND:\n"
    "s_endpgm\n"
    ".Lk_L0A:\n"
    "s_waitcnt vmcnt(0)\n"
    "s_cmp_eq_u32 s34, 3\n"
    "s_cbranch_scc0 .Lk_L0A1\n"
    "v_add_u32 v21, 0x7f4a7c15, v21\n"
    "s_getreg_b32 s46, hwreg(HW_REG_XCC_ID, 2, 1)\n"
    "s_lshl_b32 s46, s46, 1\n"
    "s_sub_u32 s46, 1, s46\n"
    "v_mul_lo_u32 v23, v23, s46\n"
    "v_ashrrev_i32 v23, 4, v23\n"
    "v_add_u32 v23, 0x80, v23\n"
    "v_add_u32 v23, v23, v21\n"
    "s_mov_b64 exec, 0xff\n"
    "global_store_dword v22, v23, s[28:29] offset:4 sc1\n"
    "s_mov_b64 exec, -1\n"
    ".Lk_L0A1:\n"
    "s_branch .Lk_L0AR\n"
    ".Lk_L0D:\n"
    "s_cmp_eq_u32 s38, 0\n"
    "s_cbranch_scc1 .Lk_SETUP\n"
    ".Lk_SR0:\n"
    "s_branch .Lk_L0DR\n"
    ".Lk_L0C:\n"
    "v_mov_b32 v1, 0\n"
    "ds_read_b64 v[2:3], v1 offset:13904\n"
    "s_memrealtime s[62:63]\n"
    "s_getreg_b32 s46, hwreg(HW_REG_XCC_ID, 0, 2)\n"
    "s_lshl_b32 s46, s46, 14\n"
    "s_add_u32 s46, s46, 0x54000\n"
    "s_waitcnt lgkmcnt(0)\n"
    "v_readfirstlane_b32 s47, v2\n"
    "v_readfirstlane_b32 s59, v3\n"
    "s_mov_b32 s97, s62\n"
    "s_and_b32 s47, s47, 0x2000\n"
    "s_and_b32 s59, s59, 0x2000\n"
    "s_add_u32 s42, s28, s47\n"
    "s_addc_u32 s43, s29, 0\n"
    "s_add_u32 s50, s42, s46\n"
    "s_addc_u32 s51, s43, 0\n"
    "s_cmp_eq_u32 s38, 2\n"
    "s_cbranch_scc1 .Lk_SETUP\n"
    "s_branch .Lk_SR2\n"
    ".Lk_L0B:\n"
    "s_cmp_eq_u32 s34, 3\n"
    "s_cbranch_scc0 .Lk_L0B1\n"
    "s_lshl_b32 s46, s31, 11\n"
    "s_add_u32 s46, s46, 0x74000\n"
    "v_lshl_add_u32 v1, v0, 4, s46\n"
    "s_movk_i32 s39, 0x4000\n"
    ".Lk_P_F:\n"
    "global_load_dwordx4 v[2:5], v1, s[28:29] sc1\n"
    "global_load_dwordx4 v[6:9], v1, s[28:29] offset:1024 sc1\n"
    "s_mov_b32 s46, 0x7f4a7c15\n"
    "s_waitcnt vmcnt(0)\n"
    "v_sub_u32 v2, v3, v2\n"
    "v_sub_u32 v4, v5, v4\n"
    "v_sub_u32 v6, v7, v6\n"
    "v_sub_u32 v8, v9, v8\n"
    "v_subrev_u32 v2, s46, v2\n"
    "v_subrev_u32 v4, s46, v4\n"
    "v_subrev_u32 v6, s46, v6\n"
    "v_subrev_u32 v8, s46, v8\n"
    "v_add3_u32 v3, v2, v4, v6\n"
    "v_or3_b32 v2, v2, v4, v6\n"
    "v_add_u32 v3, v3, v8\n"
    "v_or_b32 v2, v2, v8\n"
    "s_nop 0\n"
    "v_add_u32_dpp v3, v3, v3 quad_perm:[1,0,3,2] row_mask:0xf bank_mask:0xf\n"
    "v_cmp_lt_u32 vcc, 0xff, v2\n"
    "s_cbranch_vccnz .Lk_R_F\n"
    ".Lk_D_F:\n"
    "v_add_u32_dpp v3, v3, v3 quad_perm:[2,3,0,1] row_mask:0xf bank_mask:0xf\n"
    "s_nop 1\n"
    "v_add_u32_dpp v3, v3, v3 row_ror:4 row_mask:0xf bank_mask:0xf\n"
    "s_nop 1\n"
    "v_add_u32_dpp v3, v3, v3 row_ror:8 row_mask:0xf bank_mask:0xf\n"
    "s_nop 1\n"
    "v_add_u32_dpp v3, v3, v3 row_bcast:15 row_mask:0xa bank_mask:0xf\n"
    "s_nop 1\n"
    "v_add_u32_dpp v3, v3, v3 row_bcast:31 row_mask:0xc bank_mask:0xf\n"
    "s_getreg_b32 s46, hwreg(HW_REG_XCC_ID, 2, 1)\n"
    "s_lshl_b32 s46, s46, 13\n"
    "v_mov_b32 v1, 0\n"
    "v_readlane_b32 s47, v3, 63\n"
    "s_cmp_lt_u32 s47, 0x7f80\n"
    "s_cselect_b32 s47, 0x2000, 0\n"
    "s_xor_b32 s46, s46, s47\n"
    "v_mov_b32 v2, s46\n"
    "v_mov_b32 v3, s47\n"
    "ds_write_b64 v1, v[2:3] offset:13904\n"
    "s_waitcnt lgkmcnt(0)\n"
    ".Lk_L0B1:\n"
    "s_cmp_eq_u32 s38, 1\n"
    "s_cbranch_scc0 .Lk_MERGE\n"
    ".Lk_SETUP:\n"
    "s_cmp_eq_u32 s35, 0\n"
    "s_cbranch_scc0 .Lk_SU1\n"
    "s_xor_b32 s46, s31, 0\n"
    "s_lshl_b32 s46, s46, 2\n"
    "s_add_u32 s46, s46, s34\n"
    "s_mul_i32 s47, s46, 163840\n"
    "s_add_u32 s44, s16, s47\n"
    "s_addc_u32 s45, s17, 0\n"
    "s_lshl_b32 s47, s32, 6\n"
    "v_lshrrev_b32 v4, 4, v0\n"
    "v_and_b32 v5, 15, v0\n"
    "v_lshl_add_u32 v1, v5, 2, s47\n"
    "v_mul_u32_u24 v2, 5120, v4\n"
    "v_add_u32 v1, v1, v2\n"
    "global_load_dword v60, v1, s[44:45] offset:0 nt\n"
    "global_load_dword v61, v1, s[44:45] offset:1280 nt\n"
    "global_load_dword v62, v1, s[44:45] offset:2560 nt\n"
    "global_load_dword v63, v1, s[44:45] offset:3840 nt\n"
    "s_add_u32 s44, s44, 20480\n"
    "s_addc_u32 s45, s45, 0\n"
    "global_load_dword v64, v1, s[44:45] offset:0 nt\n"
    "global_load_dword v65, v1, s[44:45] offset:1280 nt\n"
    "global_load_dword v66, v1, s[44:45] offset:2560 nt\n"
    "global_load_dword v67, v1, s[44:45] offset:3840 nt\n"
    "s_add_u32 s44, s44, 20480\n"
    "s_addc_u32 s45, s45, 0\n"
    "global_load_dword v68, v1, s[44:45] offset:0 nt\n"
    "global_load_dword v69, v1, s[44:45] offset:1280 nt\n"
    "global_load_dword v70, v1, s[44:45] offset:2560 nt\n"
    "global_load_dword v71, v1, s[44:45] offset:3840 nt\n"
    "s_add_u32 s44, s44, 20480\n"
    "s_addc_u32 s45, s45, 0\n"
    "global_load_dword v72, v1, s[44:45] offset:0 nt\n"
    "global_load_dword v73, v1, s[44:45] offset:1280 nt\n"
    "global_load_dword v74, v1, s[44:45] offset:2560 nt\n"
    "global_load_dword v75, v1, s[44:45] offset:3840 nt\n"
    "s_add_u32 s44, s44, 20480\n"
    "s_addc_u32 s45, s45, 0\n"
    "global_load_dword v76, v1, s[44:45] offset:0 nt\n"
    "global_load_dword v77, v1, s[44:45] offset:1280 nt\n"
    "global_load_dword v78, v1, s[44:45] offset:2560 nt\n"
    "global_load_dword v79, v1, s[44:45] offset:3840 nt\n"
    "s_add_u32 s44, s44, 20480\n"
    "s_addc_u32 s45, s45, 0\n"
    "global_load_dword v80, v1, s[44:45] offset:0 nt\n"
    "global_load_dword v81, v1, s[44:45] offset:1280 nt\n"
    "global_load_dword v82, v1, s[44:45] offset:2560 nt\n"
    "global_load_dword v83, v1, s[44:45] offset:3840 nt\n"
    "s_add_u32 s44, s44, 20480\n"
    "s_addc_u32 s45, s45, 0\n"
    "global_load_dword v84, v1, s[44:45] offset:0 nt\n"
    "global_load_dword v85, v1, s[44:45] offset:1280 nt\n"
    "global_load_dword v86, v1, s[44:45] offset:2560 nt\n"
    "global_load_dword v87, v1, s[44:45] offset:3840 nt\n"
    "s_add_u32 s44, s44, 20480\n"
    "s_addc_u32 s45, s45, 0\n"
    "global_load_dword v88, v1, s[44:45] offset:0 nt\n"
    "global_load_dword v89, v1, s[44:45] offset:1280 nt\n"
    "global_load_dword v90, v1, s[44:45] offset:2560 nt\n"
    "global_load_dword v91, v1, s[44:45] offset:3840 nt\n"
    "s_lshr_b32 s47, s46, 1\n"
    "s_lshl_b32 s47, s47, 14\n"
    "s_and_b32 s46, s46, 1\n"
    "s_lshl_b32 s46, s46, 12\n"
    "s_add_u32 s46, s46, s47\n"
    "v_lshrrev_b32 v6, 1, v5\n"
    "v_lshl_add_u32 v14, v6, 6, s46\n"
    "v_lshl_add_u32 v14, v4, 4, v14\n"
    "v_lshl_add_u32 v6, v4, 1, v0\n"
    "v_and_b32 v6, 7, v6\n"
    "v_lshl_add_u32 v163, v6, 4, s46\n"
    "s_lshl_b32 s46, s34, 8\n"
    "v_lshl_add_u32 v161, v5, 3, s46\n"
    "v_lshrrev_b32 v6, 5, v0\n"
    "v_lshl_add_u32 v161, v6, 2, v161\n"
    "v_and_b32 v6, 1, v4\n"
    "v_lshl_add_u32 v161, v6, 7, v161\n"
    "v_and_b32 v6, 31, v0\n"
    "v_lshlrev_b32 v162, 2, v6\n"
    "v_add_u32 v162, 12800, v162\n"
    "s_branch .Lk_SRET\n"
    ".Lk_SU1:\n"
    "s_cmp_eq_u32 s35, 1\n"
    "s_cbranch_scc0 .Lk_SU2\n"
    "s_mul_i32 s47, s34, 552960\n"
    "s_add_u32 s44, s18, s47\n"
    "s_addc_u32 s45, s19, 0\n"
    "s_lshl_b32 s47, s36, 6\n"
    "v_lshrrev_b32 v4, 4, v0\n"
    "v_and_b32 v5, 15, v0\n"
    "v_lshl_add_u32 v1, v5, 2, s47\n"
    "global_load_dword v115, v1, s[20:21]\n"
    "v_mul_u32_u24 v2, 13824, v4\n"
    "v_add_u32 v1, v1, v2\n"
    "v_add_u32 v2, 6912, v1\n"
    "global_load_dword v60, v1, s[44:45] offset:0 nt\n"
    "global_load_dword v61, v1, s[44:45] offset:3456 nt\n"
    "global_load_dword v62, v2, s[44:45] offset:0 nt\n"
    "global_load_dword v63, v2, s[44:45] offset:3456 nt\n"
    "s_add_u32 s44, s44, 55296\n"
    "s_addc_u32 s45, s45, 0\n"
    "global_load_dword v64, v1, s[44:45] offset:0 nt\n"
    "global_load_dword v65, v1, s[44:45] offset:3456 nt\n"
    "global_load_dword v66, v2, s[44:45] offset:0 nt\n"
    "global_load_dword v67, v2, s[44:45] offset:3456 nt\n"
    "s_add_u32 s44, s44, 55296\n"
    "s_addc_u32 s45, s45, 0\n"
    "global_load_dword v68, v1, s[44:45] offset:0 nt\n"
    "global_load_dword v69, v1, s[44:45] offset:3456 nt\n"
    "global_load_dword v70, v2, s[44:45] offset:0 nt\n"
    "global_load_dword v71, v2, s[44:45] offset:3456 nt\n"
    "s_add_u32 s44, s44, 55296\n"
    "s_addc_u32 s45, s45, 0\n"
    "global_load_dword v72, v1, s[44:45] offset:0 nt\n"
    "global_load_dword v73, v1, s[44:45] offset:3456 nt\n"
    "global_load_dword v74, v2, s[44:45] offset:0 nt\n"
    "global_load_dword v75, v2, s[44:45] offset:3456 nt\n"
    "s_add_u32 s44, s44, 55296\n"
    "s_addc_u32 s45, s45, 0\n"
    "global_load_dword v76, v1, s[44:45] offset:0 nt\n"
    "global_load_dword v77, v1, s[44:45] offset:3456 nt\n"
    "global_load_dword v78, v2, s[44:45] offset:0 nt\n"
    "global_load_dword v79, v2, s[44:45] offset:3456 nt\n"
    "s_add_u32 s44, s44, 55296\n"
    "s_addc_u32 s45, s45, 0\n"
    "global_load_dword v80, v1, s[44:45] offset:0 nt\n"
    "global_load_dword v81, v1, s[44:45] offset:3456 nt\n"
    "global_load_dword v82, v2, s[44:45] offset:0 nt\n"
    "global_load_dword v83, v2, s[44:45] offset:3456 nt\n"
    "s_add_u32 s44, s44, 55296\n"
    "s_addc_u32 s45, s45, 0\n"
    "global_load_dword v84, v1, s[44:45] offset:0 nt\n"
    "global_load_dword v85, v1, s[44:45] offset:3456 nt\n"
    "global_load_dword v86, v2, s[44:45] offset:0 nt\n"
    "global_load_dword v87, v2, s[44:45] offset:3456 nt\n"
    "s_add_u32 s44, s44, 55296\n"
    "s_addc_u32 s45, s45, 0\n"
    "global_load_dword v88, v1, s[44:45] offset:0 nt\n"
    "global_load_dword v89, v1, s[44:45] offset:3456 nt\n"
    "global_load_dword v90, v2, s[44:45] offset:0 nt\n"
    "global_load_dword v91, v2, s[44:45] offset:3456 nt\n"
    "s_add_u32 s44, s44, 55296\n"
    "s_addc_u32 s45, s45, 0\n"
    "global_load_dword v92, v1, s[44:45] offset:0 nt\n"
    "global_load_dword v93, v1, s[44:45] offset:3456 nt\n"
    "global_load_dword v94, v2, s[44:45] offset:0 nt\n"
    "global_load_dword v95, v2, s[44:45] offset:3456 nt\n"
    "s_add_u32 s44, s44, 55296\n"
    "s_addc_u32 s45, s45, 0\n"
    "global_load_dword v96, v1, s[44:45] offset:0 nt\n"
    "global_load_dword v97, v1, s[44:45] offset:3456 nt\n"
    "global_load_dword v98, v2, s[44:45] offset:0 nt\n"
    "global_load_dword v99, v2, s[44:45] offset:3456 nt\n"
    "v_lshrrev_b32 v6, 1, v5\n"
    "v_lshlrev_b32 v163, 5, v6\n"
    "v_lshl_add_u32 v163, v4, 3, v163\n"
    "v_and_b32 v6, 1, v0\n"
    "v_lshl_add_u32 v163, v6, 2, v163\n"
    "s_lshl_b32 s46, s34, 14\n"
    "s_add_u32 s46, s46, 0x40a00\n"
    "v_lshl_add_u32 v14, v163, 2, s46\n"
    "s_mul_i32 s46, s34, 160\n"
    "v_add_u32 v163, s46, v163\n"
    "s_lshl_b32 s46, s34, 8\n"
    "s_add_u32 s46, s46, 12800\n"
    "v_lshl_add_u32 v161, v5, 4, s46\n"
    "v_lshl_add_u32 v161, v4, 3, v161\n"
    "v_lshlrev_b32 v6, 3, v0\n"
    "v_add_u32 v6, 13952, v6\n"
    "v_cmp_gt_u32 vcc, 32, v0\n"
    "s_nop 1\n"
    "v_cndmask_b32 v161, v6, v161\n"
    "s_lshl_b32 s46, s34, 3\n"
    "s_add_u32 s46, s46, 13824\n"
    "v_lshrrev_b32 v7, 5, v0\n"
    "v_lshl_add_u32 v7, v7, 2, s46\n"
    "v_and_b32 v8, 31, v0\n"
    "v_cmp_eq_u32 vcc, 31, v8\n"
    "s_nop 1\n"
    "v_cndmask_b32 v116, v6, v7\n"
    "v_lshlrev_b32 v162, 4, v5\n"
    "v_mov_b32 v112, 0\n"
    "v_mov_b32 v113, 0\n"
    "v_mov_b32 v100, s48\n"
    "v_mov_b32 v101, s48\n"
    "v_mov_b32 v102, s48\n"
    "v_mov_b32 v103, s48\n"
    "v_mov_b32 v104, s48\n"
    "v_mov_b32 v105, s48\n"
    "v_mov_b32 v106, s48\n"
    "v_mov_b32 v107, s48\n"
    "s_cmp_ge_u32 s34, 2\n"
    "s_cbranch_scc0 .Lk_SM1X\n"
    "s_add_u32 s44, s34, 6\n"
    "s_mul_i32 s46, s36, 40960\n"
    "s_lshl_b32 s47, s44, 7\n"
    "s_add_u32 s46, s46, s47\n"
    "v_lshrrev_b32 v4, 3, v0\n"
    "v_mul_u32_u24 v4, 1280, v4\n"
    "v_and_b32 v5, 7, v0\n"
    "v_lshl_add_u32 v4, v5, 4, v4\n"
    "v_add_u32 v4, s46, v4\n"
    "s_lshl_b32 s47, s44, 12\n"
    "s_add_u32 m0, s47, 14592\n"
    "s_nop 0\n"
    "global_load_lds_dwordx4 v4, s[22:23] nt\n"
    "v_add_u32 v4, 10240, v4\n"
    "s_add_u32 m0, m0, 1024\n"
    "s_nop 0\n"
    "global_load_lds_dwordx4 v4, s[22:23] nt\n"
    "v_add_u32 v4, 10240, v4\n"
    "s_add_u32 m0, m0, 1024\n"
    "s_nop 0\n"
    "global_load_lds_dwordx4 v4, s[22:23] nt\n"
    "v_add_u32 v4, 10240, v4\n"
    "s_add_u32 m0, m0, 1024\n"
    "s_nop 0\n"
    "global_load_lds_dwordx4 v4, s[22:23] nt\n"
    ".Lk_PFL5:\n"
    "s_memrealtime s[44:45]\n"
    "s_waitcnt lgkmcnt(0)\n"
    "s_sub_u32 s44, s44, s96\n"
    "s_cmp_lt_u32 s44, 1000\n"
    "s_cbranch_scc0 .Lk_PFX5\n"
    "s_sleep 2\n"
    "s_branch .Lk_PFL5\n"
    ".Lk_PFX5:\n"
    "s_and_b32 s44, s34, 1\n"
    "s_lshl_b32 s44, s44, 2\n"
    "s_mul_i32 s46, s36, 40960\n"
    "s_lshl_b32 s47, s44, 7\n"
    "s_add_u32 s46, s46, s47\n"
    "v_lshrrev_b32 v4, 3, v0\n"
    "v_mul_u32_u24 v4, 1280, v4\n"
    "v_and_b32 v5, 7, v0\n"
    "v_lshl_add_u32 v4, v5, 4, v4\n"
    "v_add_u32 v4, s46, v4\n"
    "s_lshl_b32 s47, s44, 12\n"
    "s_add_u32 m0, s47, 14592\n"
    "s_nop 0\n"
    "global_load_lds_dwordx4 v4, s[22:23] nt\n"
    "v_add_u32 v4, 10240, v4\n"
    "s_add_u32 m0, m0, 1024\n"
    "s_nop 0\n"
    "global_load_lds_dwordx4 v4, s[22:23] nt\n"
    "v_add_u32 v4, 10240, v4\n"
    "s_add_u32 m0, m0, 1024\n"
    "s_nop 0\n"
    "global_load_lds_dwordx4 v4, s[22:23] nt\n"
    "v_add_u32 v4, 10240, v4\n"
    "s_add_u32 m0, m0, 1024\n"
    "s_nop 0\n"
    "global_load_lds_dwordx4 v4, s[22:23] nt\n"
    "v_add_u32 v4, -30592, v4\n"
    "s_add_u32 m0, m0, 1024\n"
    "s_nop 0\n"
    "global_load_lds_dwordx4 v4, s[22:23] nt\n"
    "v_add_u32 v4, 10240, v4\n"
    "s_add_u32 m0, m0, 1024\n"
    "s_nop 0\n"
    "global_load_lds_dwordx4 v4, s[22:23] nt\n"
    "v_add_u32 v4, 10240, v4\n"
    "s_add_u32 m0, m0, 1024\n"
    "s_nop 0\n"
    "global_load_lds_dwordx4 v4, s[22:23] nt\n"
    "v_add_u32 v4, 10240, v4\n"
    "s_add_u32 m0, m0, 1024\n"
    "s_nop 0\n"
    "global_load_lds_dwordx4 v4, s[22:23] nt\n"
    "v_add_u32 v4, -30592, v4\n"
    "s_add_u32 m0, m0, 1024\n"
    "s_nop 0\n"
    "global_load_lds_dwordx4 v4, s[22:23] nt\n"
    "v_add_u32 v4, 10240, v4\n"
    "s_add_u32 m0, m0, 1024\n"
    "s_nop 0\n"
    "global_load_lds_dwordx4 v4, s[22:23] nt\n"
    "v_add_u32 v4, 10240, v4\n"
    "s_add_u32 m0, m0, 1024\n"
    "s_nop 0\n"
    "global_load_lds_dwordx4 v4, s[22:23] nt\n"
    "v_add_u32 v4, 10240, v4\n"
    "s_add_u32 m0, m0, 1024\n"
    "s_nop 0\n"
    "global_load_lds_dwordx4 v4, s[22:23] nt\n"
    "v_add_u32 v4, -30592, v4\n"
    "s_add_u32 m0, m0, 1024\n"
    "s_nop 0\n"
    "global_load_lds_dwordx4 v4, s[22:23] nt\n"
    "v_add_u32 v4, 10240, v4\n"
    "s_add_u32 m0, m0, 1024\n"
    "s_nop 0\n"
    "global_load_lds_dwordx4 v4, s[22:23] nt\n"
    "v_add_u32 v4, 10240, v4\n"
    "s_add_u32 m0, m0, 1024\n"
    "s_nop 0\n"
    "global_load_lds_dwordx4 v4, s[22:23] nt\n"
    "v_add_u32 v4, 10240, v4\n"
    "s_add_u32 m0, m0, 1024\n"
    "s_nop 0\n"
    "global_load_lds_dwordx4 v4, s[22:23] nt\n"
    ".Lk_SM1X:\n"
    "s_branch .Lk_SRET\n"
    ".Lk_SU2:\n"
    "s_lshl_b32 s47, s36, 5\n"
    "v_and_b32 v5, 3, v0\n"
    "v_lshl_add_u32 v121, v5, 3, s47\n"
    "global_load_dwordx2 v[62:63], v121, s[24:25]\n"
    "global_load_dwordx2 v[64:65], v121, s[8:9]\n"
    "global_load_dword v9, v121, s[26:27]\n"
    "s_add_u32 s47, s47, 0x54000\n"
    "v_lshl_add_u32 v15, v5, 3, s47\n"
    "v_bfe_u32 v6, v0, 2, 3\n"
    "v_lshl_add_u32 v15, v6, 13, v15\n"
    "s_lshr_b32 s46, s31, 1\n"
    "s_mul_i32 s46, s46, 15744\n"
    "s_lshl_b32 s47, s36, 6\n"
    "s_add_u32 s46, s46, s47\n"
    "s_add_u32 s46, s46, 0x40000\n"
    "v_lshl_add_u32 v120, v5, 4, s46\n"
    "v_bfe_u32 v6, v0, 2, 2\n"
    "v_mul_u32_u24 v6, 640, v6\n"
    "v_add_u32 v120, v120, v6\n"
    "v_mul_u32_u24 v160, 0x5555, v5\n"
    "v_mov_b32 v130, 0\n"
    "v_mov_b32 v131, 0\n"
    "v_mov_b32 v68, 0x3f80\n"
    "v_mov_b32 v69, 0x3f80\n"
    "v_mov_b32 v70, 0x3f80\n"
    "v_mov_b32 v71, 0x3f80\n"
    ".Lk_SRET:\n"
    "s_cmp_eq_u32 s38, 1\n"
    "s_mov_b32 s46, s38\n"
    "s_mov_b32 s38, 7\n"
    "s_cbranch_scc1 .Lk_MERGE\n"
    "s_cmp_eq_u32 s46, 0\n"
    "s_cbranch_scc1 .Lk_SR0\n"
    "s_branch .Lk_SR2\n"
    ".Lk_B2F:\n"
    "s_waitcnt vmcnt(0)\n"
    "s_add_u32 s46, s34, s37\n"
    "s_cmp_eq_u32 s46, 4\n"
    "s_cbranch_scc0 .Lk_B2\n"
    "s_mov_b64 exec, 0xff\n"
    "global_store_dword v22, v21, s[28:29] sc1\n"
    "s_mov_b64 exec, -1\n"
    "s_branch .Lk_B2\n"
    ".Lk_R_HF:\n"
    "s_sub_u32 s39, s39, 1\n"
    "s_cmp_lg_u32 s39, 0\n"
    "s_cbranch_scc1 .Lk_P_HF\n"
    "s_branch .Lk_D_HF\n"
    ".Lk_R_HU:\n"
    "s_sub_u32 s39, s39, 1\n"
    "s_cmp_lg_u32 s39, 0\n"
    "s_cbranch_scc1 .Lk_P_HU\n"
    "s_branch .Lk_D_HU\n"
    ".Lk_R_X:\n"
    "s_sub_u32 s39, s39, 1\n"
    "s_cmp_lg_u32 s39, 0\n"
    "s_cbranch_scc1 .Lk_P_X\n"
    "s_branch .Lk_D_X\n"
    ".Lk_R_Q:\n"
    "s_sub_u32 s39, s39, 1\n"
    "s_cmp_lg_u32 s39, 0\n"
    "s_cbranch_scc1 .Lk_P_Q\n"
    "s_branch .Lk_D_Q\n"
    ".Lk_R_M:\n"
    "s_sub_u32 s39, s39, 1\n"
    "s_cmp_lg_u32 s39, 0\n"
    "s_cbranch_scc1 .Lk_P_M\n"
    "s_branch .Lk_D_M\n"
    ".Lk_R_W:\n"
    "v_mov_b32 v164, 0\n"
    "v_mov_b32 v165, 0\n"
    "v_mov_b32 v166, 0\n"
    "v_mov_b32 v167, 0\n"
    "s_sub_u32 s39, s39, 1\n"
    "s_cmp_lg_u32 s39, 0\n"
    "s_cbranch_scc1 .Lk_P_W\n"
    "s_branch .Lk_D_W\n"
    ".Lk_R_Z:\n"
    "s_mov_b32 exec_lo, 0x03ff03ff\n"
    "s_mov_b32 exec_hi, 0x03ff03ff\n"
    "s_sub_u32 s39, s39, 1\n"
    "s_cmp_lg_u32 s39, 0\n"
    "s_cbranch_scc1 .Lk_P_Z\n"
    "s_branch .Lk_D_Z\n"
    ".Lk_R_Y:\n"
    "s_sub_u32 s39, s39, 1\n"
    "s_cmp_lg_u32 s39, 0\n"
    "s_cbranch_scc1 .Lk_P_Y\n"
    "s_branch .Lk_D_Y\n"
    ".Lk_R_H:\n"
    "s_sub_u32 s39, s39, 1\n"
    "s_cmp_lg_u32 s39, 0\n"
    "s_cbranch_scc1 .Lk_P_H\n"
    "s_branch .Lk_D_H\n"
    ".Lk_R_F:\n"
    "s_sub_u32 s39, s39, 1\n"
    "s_cmp_lg_u32 s39, 0\n"
    "s_cbranch_scc1 .Lk_P_F\n"
    "s_branch .Lk_D_F\n"
    : "+v"(tid)
    : "s"(__builtin_amdgcn_kernarg_segment_ptr()), "s"(blockIdx.x), "s"((unsigned)(size_t)(__attribute__((address_space(3))) char*)lds)
    : "v1", "v2", "v3", "v4", "v5", "v6", "v7", "v8", "v9", "v10", "v11", "v12", "v13", "v14", "v15", "v16", "v17", "v18", "v19", "v20", "v21", "v22", "v23", "v24", "v25", "v26", "v27", "v28", "v29", "v30", "v31", "v32", "v33", "v34", "v35", "v36", "v37", "v38", "v39", "v40", "v41", "v42", "v43", "v44", "v45", "v46", "v47", "v48", "v49", "v50", "v51", "v52", "v53", "v54", "v55", "v56", "v57", "v58", "v59", "v60", "v61", "v62", "v63", "v64", "v65", "v66", "v67", "v68", "v69", "v70", "v71", "v72", "v73", "v74", "v75", "v76", "v77", "v78", "v79", "v80", "v81", "v82", "v83", "v84", "v85", "v86", "v87", "v88", "v89", "v90", "v91", "v92", "v93", "v94", "v95", "v96", "v97", "v98", "v99", "v100", "v101", "v102", "v103", "v104", "v105", "v106", "v107", "v108", "v109", "v110", "v111", "v112", "v113", "v114", "v115", "v116", "v117", "v118", "v119", "v120", "v121", "v122", "v123", "v124", "v125", "v126", "v127", "v128", "v129", "v130", "v131", "v132", "v133", "v134", "v135", "v136", "v137", "v138", "v139", "v140", "v141", "v142", "v143", "v144", "v145", "v146", "v147", "v148", "v149", "v150", "v151", "v152", "v153", "v154", "v155", "v156", "v157", "v158", "v159", "v160", "v161", "v162", "v163", "v164", "v165", "v166", "v167", "s8", "s9", "s10", "s11", "s12", "s13", "s14", "s15", "s16", "s17", "s18", "s19", "s20", "s21", "s22", "s23", "s24", "s25", "s26", "s27", "s28", "s29", "s30", "s31", "s32", "s33", "s34", "s35", "s36", "s37", "s38", "s39", "s40", "s41", "s42", "s43", "s44", "s45", "s46", "s47", "s48", "s49", "s50", "s51", "s52", "s53", "s54", "s55", "s56", "s57", "s58", "s59", "s60", "s61", "s62", "s63", "s64", "s65", "s66", "s67", "s68", "s69", "s70", "s71", "s72", "s73", "s74", "s75", "s76", "s77", "s78", "s79", "s80", "s81", "s82", "s83", "s84", "s85", "s86", "s87", "s88", "s89", "s90", "s91", "s92", "s93", "s94", "s95", "s96", "s97", "s98", "s99", "vcc", "scc", "memory");
}

extern "C" void kernel_launch(void* const* d_in, const int* in_sizes, int n_in, void* d_out, int out_size,
                              void* d_ws, size_t ws_size, hipStream_t stream) {
  char* ws = (char*)(((uintptr_t)d_ws + 0x1fffff) & ~(uintptr_t)0x1fffff);
  fused_kernel<<<256, 768, 0, stream>>>(d_in[0], d_in[1], d_in[2], d_in[3], d_in[4], d_in[6], d_in[7], d_in[8],
                                        d_in[9], d_out, ws);
}
```
